```python
import torch

def _softmax(x, axis=-1):
    return torch.softmax(x, dim=axis)

def _gelu(x):
    return torch.nn.functional.gelu(x, approximate='tanh')

def _silu(x):
    return torch.nn.functional.silu(x)

def _sigmoid(x):
    return torch.sigmoid(x)

def _layernorm(x, axis=-1, eps=1e-05):
    m = x.mean(dim=axis, keepdim=True)
    v = x.var(dim=axis, keepdim=True, unbiased=False)
    return (x - m) / torch.sqrt(v + eps)

def _scatter_add(tgt, idx, src, axis=0):
    return tgt.clone().index_add_(axis, idx, src)

def _segment_sum(src, seg, n):
    out = torch.zeros((n,) + src.shape[1:], dtype=src.dtype)
    return out.index_add_(0, seg, src)

def _pad(x, pw):
    flat = tuple((v for pair in reversed(pw) for v in pair))
    return torch.nn.functional.pad(x, flat)
kernel_name = 'adaLN_cond__ssm_conv_h3__moe_arch_mod__prompt'

def setup_inputs():

    _w = lambda a: (lambda t: t.to(torch.bfloat16) if t.is_floating_point() else t)(torch.as_tensor(a))

    def _A_setup():
        B, S, D = (1, 2048, 512)
        ks = 4
        return {'q': _w((lambda *s: torch.randn(*s, dtype=torch.bfloat16) * 0.1)(B, S, D)), 'k': _w((lambda *s: torch.randn(*s, dtype=torch.bfloat16) * 3.2)(B, S, D)), 'v': _w((lambda *s: torch.randn(*s, dtype=torch.bfloat16) * 3.2)(B, S, D)), 'shift_k': _w((lambda *s: torch.randn(*s, dtype=torch.bfloat16) * 3.2)(D, ks) * 0.5), 'ssm_k': _w((lambda *s: torch.randn(*s, dtype=torch.bfloat16) * 3.2)(D, S) * (1.0 / S ** 0.5)), 'ssm_D': _w((lambda *s: torch.randn(*s, dtype=torch.bfloat16) * 3.2)(D))}
    _A_in = _A_setup()

    def _B_setup():
        B, S, D, F, C = (1, 2048, 512, 1792, 8)
        return {'x': _w((lambda *s: torch.randn(*s, dtype=torch.bfloat16) * 0.1)(B, S, D)), 'w_r': _w((lambda *s: torch.randn(*s, dtype=torch.bfloat16) * 0.4)(D)), 'w_gate': _w((lambda *s: torch.randn(*s, dtype=torch.bfloat16) * 0.4)(D, F)), 'w_up': _w((lambda *s: torch.randn(*s, dtype=torch.bfloat16) * 0.4)(D, F)), 'w_down': _w((lambda *s: torch.randn(*s, dtype=torch.bfloat16) * 0.4)(F, D))}
    _B_in = _B_setup()
    return {'A_q': _A_in['q'], 'A_k': _A_in['k'], 'A_v': _A_in['v'], 'A_shift_k': _A_in['shift_k'], 'A_ssm_k': _A_in['ssm_k'], 'A_ssm_D': _A_in['ssm_D'], 'B_x': _B_in['x'], 'B_w_r': _B_in['w_r'], 'B_w_gate': _B_in['w_gate'], 'B_w_up': _B_in['w_up'], 'B_w_down': _B_in['w_down'], 'c': _w((lambda *s: torch.randn(*s, dtype=torch.bfloat16) * 0.2)(1, 512) * 8.0), 'w_mod': _w((lambda *s: torch.randn(*s, dtype=torch.bfloat16) * 0.2)(512, 6 * 512) * 512 ** (-0.5) * 8.0)}

def reference(A_q, A_k, A_v, A_shift_k, A_ssm_k, A_ssm_D, B_x, B_w_r, B_w_gate, B_w_up, B_w_down, c, w_mod):

    def _A_ref(q, k, v, shift_k, ssm_k, ssm_D):
        B, S, D = q.shape
        ks = shift_k.shape[-1]
        k_pad = _pad(k, ((0, 0), (ks - 1, 0), (0, 0)))
        k_shift = torch.zeros_like(k)
        for t in range(ks):
            k_shift = k_shift + k_pad[:, t:t + S, :] * shift_k[:, ks - 1 - t][None, None, :]
        kv = k_shift * v
        L = 2 * S
        kv_pad = _pad(kv, ((0, 0), (0, S), (0, 0)))
        kf_pad = _pad(ssm_k, ((0, 0), (0, S)))
        kv_f = (lambda x, axes=(-2, -1): torch.fft.rfft2(x.float(), dim=axes))(kv_pad[:, None, :, :], axes=(1, 2))
        kf_f = (lambda x, axes=(-2, -1): torch.fft.rfft2(x.float(), dim=axes))(kf_pad[:, None, :], axes=(1, 2))
        kf_f = (lambda x, *d: x.permute(*d))(kf_f, 1, 2, 0)
        y_f = kv_f * kf_f[None, :, :, :]
        y = (lambda x, s, axes=(-2, -1): torch.fft.irfft2(x, s=s, dim=axes).to(torch.bfloat16))(y_f, (1, L), axes=(1, 2))[:, 0, :S, :]
        y = y + kv * ssm_D[None, None, :]
        return q * y

    def _B_ref(x, w_r, w_gate, w_up, w_down):
        B, S, D = x.shape
        C = 8
        r = x @ w_r
        idx = torch.argsort(-r, axis=-1)[:, :C]
        idx_e = (lambda x, n, axis: torch.repeat_interleave(x, n, dim=axis))(idx.reshape(B, C, 1), D, axis=2)
        x_sel = (lambda x, i, axis: torch.take_along_dim(x, i, dim=axis))(x, idx_e, axis=1)
        g = _silu(x_sel @ w_gate)
        u = x_sel @ w_up
        y = g * u @ w_down
        r_sel = _sigmoid((lambda x, i, axis: torch.take_along_dim(x, i, dim=axis))(r, idx, axis=1))
        y = y * r_sel.reshape(B, C, 1)
        b_off = (torch.arange(B) * S).reshape(B, 1)
        flat_idx = (idx + b_off).reshape(B * C)
        zero = (x * 0.0).reshape(B * S, D)
        upd = _scatter_add(zero, flat_idx, y.reshape(B * C, D), axis=0)
        return x + upd.reshape(B, S, D)
    x = A_q
    mod = (_silu(c) @ w_mod).reshape(c.shape[0], 6, 1, -1)
    sa, ca, ga, sb, cb, gb = (mod[:, 0].clone(), mod[:, 1].clone(), mod[:, 2].clone(), mod[:, 3].clone(), mod[:, 4].clone(), mod[:, 5].clone())
    h = _layernorm(x, axis=-1) * (1 + ca) + sa
    x = x + ga * _A_ref(h, A_k, A_v, A_shift_k, A_ssm_k, A_ssm_D)
    h = _layernorm(x, axis=-1) * (1 + cb) + sb
    x = x + gb * _B_ref(h, B_w_r, B_w_gate, B_w_up, B_w_down)
    return x
```

```cpp
#include <hip/hip_runtime.h>
#include <hip/hip_bf16.h>
#include <stdint.h>

typedef unsigned short u16;
typedef unsigned int u32;
typedef short bf16x8 __attribute__((ext_vector_type(8)));
typedef float f32x4 __attribute__((ext_vector_type(4)));
typedef u32 u32x4 __attribute__((ext_vector_type(4)));
typedef u32 u32x2 __attribute__((ext_vector_type(2)));
typedef u32x4 u32x4_u __attribute__((aligned(2)));

constexpr int S = 2048, D = 512, F = 1792;

__device__ __forceinline__ float bf2f(u32 h) { return __uint_as_float(h << 16); }
__device__ __forceinline__ float lo2f(u32 w) { return __uint_as_float(w << 16); }
__device__ __forceinline__ float hi2f(u32 w) { return __uint_as_float(w & 0xffff0000u); }
__device__ __forceinline__ u32 f2bf(float x) { __bf16 b = (__bf16)x; return (u32)__builtin_bit_cast(u16, b); }
__device__ __forceinline__ float rb(float x) { return (float)(__bf16)x; }
__device__ __forceinline__ u32 pack2(float lo, float hi) { return f2bf(lo) | (f2bf(hi) << 16); }

__device__ __forceinline__ float wave_sum(float v) {
#pragma unroll
  for (int o = 32; o > 0; o >>= 1) v += __shfl_xor(v, o, 64);
  return v;
}

__global__ __launch_bounds__(256) void k_prep(const u16* __restrict__ k, const u16* __restrict__ v,
                                              const u16* __restrict__ shk, const u16* __restrict__ c,
                                              const u16* __restrict__ wmod, u16* __restrict__ kvT,
                                              u16* __restrict__ mod) {
  const int tid = threadIdx.x, lane = tid & 63, wv = tid >> 6;
  if (blockIdx.x < 256) {
    const int cg = blockIdx.x & 7, rgp = blockIdx.x >> 3;
    const int rg = lane >> 3, cp = (lane & 7) + 8 * wv;
    const int d0 = cg * 64 + cp * 2, s0 = rgp * 64 + rg * 8;
    u32 kk[11], vv[8];
#pragma unroll
    for (int j = 0; j < 11; ++j) {
      const int s = s0 - 3 + j;
      kk[j] = (s >= 0) ? *(const u32*)(k + (size_t)s * D + d0) : 0u;
    }
#pragma unroll
    for (int j = 0; j < 8; ++j) vv[j] = *(const u32*)(v + (size_t)(s0 + j) * D + d0);
    const u32x4 sk = *(const u32x4*)(shk + d0 * 4);
    const float a0 = lo2f(sk.x), a1 = hi2f(sk.x), a2 = lo2f(sk.y), a3 = hi2f(sk.y);
    const float b0 = lo2f(sk.z), b1 = hi2f(sk.z), b2 = lo2f(sk.w), b3 = hi2f(sk.w);
    u32 oa[8], ob[8];
#pragma unroll
    for (int j = 0; j < 8; ++j) {
      float t = rb(lo2f(kk[j]) * a3);
      t = rb(t + rb(lo2f(kk[j + 1]) * a2));
      t = rb(t + rb(lo2f(kk[j + 2]) * a1));
      t = rb(t + rb(lo2f(kk[j + 3]) * a0));
      oa[j] = f2bf(t * lo2f(vv[j]));
      float u = rb(hi2f(kk[j]) * b3);
      u = rb(u + rb(hi2f(kk[j + 1]) * b2));
      u = rb(u + rb(hi2f(kk[j + 2]) * b1));
      u = rb(u + rb(hi2f(kk[j + 3]) * b0));
      ob[j] = f2bf(u * hi2f(vv[j]));
    }
    u32x4 wa = {oa[7] | (oa[6] << 16), oa[5] | (oa[4] << 16), oa[3] | (oa[2] << 16), oa[1] | (oa[0] << 16)};
    u32x4 wb = {ob[7] | (ob[6] << 16), ob[5] | (ob[4] << 16), ob[3] | (ob[2] << 16), ob[1] | (ob[0] << 16)};
    *(u32x4*)(kvT + (size_t)d0 * S + s0) = wa;
    *(u32x4*)(kvT + (size_t)(d0 + 1) * S + s0) = wb;
  } else {
    __shared__ float red[64][33];
    const int mb = blockIdx.x - 256;
    const int cc = tid & 3, rg = tid >> 2;
    float acc[8] = {0, 0, 0, 0, 0, 0, 0, 0};
    u32x4 w[8];
#pragma unroll
    for (int j = 0; j < 8; ++j) w[j] = *(const u32x4*)(wmod + (size_t)(rg * 8 + j) * 3072 + mb * 32 + cc * 8);
    const u32x4 cw = *(const u32x4*)(c + rg * 8);
#pragma unroll
    for (int j = 0; j < 8; ++j) {
      const u32 cwj = cw[j >> 1];
      const float cv = (j & 1) ? hi2f(cwj) : lo2f(cwj);
      const float sc = rb(cv / (1.f + __expf(-cv)));
#pragma unroll
      for (int e = 0; e < 4; ++e) {
        acc[2 * e] += sc * lo2f(w[j][e]);
        acc[2 * e + 1] += sc * hi2f(w[j][e]);
      }
    }
#pragma unroll
    for (int e = 0; e < 8; ++e) red[rg][cc * 8 + e] = acc[e];
    __syncthreads();
    if (tid < 32) {
      float s = 0;
      for (int r = 0; r < 64; ++r) s += red[r][tid];
      mod[mb * 32 + tid] = (u16)f2bf(s);
    }
  }
}

__global__ __launch_bounds__(256) void k_conv(const u16* __restrict__ kvT, const u16* __restrict__ ssmk,
                                              const u16* __restrict__ ssmD, u16* __restrict__ zT) {
  __shared__ u32x4 U[2][288];
  __shared__ f32x4 R[4][4][64];
  const int tid = threadIdx.x, lane = tid & 63, wv = tid >> 6;
  const int ch = wv >> 1, half = __builtin_amdgcn_readfirstlane(wv & 1);
  const int d = blockIdx.x * 2 + ch;
  {
    const u32x4 a = *(const u32x4*)(kvT + (size_t)(blockIdx.x * 2) * S + tid * 8);
    const u32x4 b = *(const u32x4*)(kvT + (size_t)(blockIdx.x * 2 + 1) * S + tid * 8);
    U[0][30 + tid] = a;
    U[1][30 + tid] = b;
    if (tid < 30) { U[0][tid] = (u32x4){0, 0, 0, 0}; U[1][tid] = (u32x4){0, 0, 0, 0}; }
  }
  const int r = lane & 15, h = lane >> 4;
  const u16* kp = ssmk + (size_t)d * S;
  u32x4 A[4][8];
#pragma unroll
  for (int w = 0; w < 4; ++w)
#pragma unroll
    for (int p = 0; p < 8; ++p) {
      const int t = 8 * p + 4 * half + w;
      int off = 32 * t - 15 + r + 8 * h;
      if (w == 0 && p == 0) {
        u32 e[8];
#pragma unroll
        for (int j = 0; j < 8; ++j) e[j] = (off + j >= 0) ? (u32)kp[off + j < 0 ? 0 : off + j] : 0u;
        A[w][p] = (u32x4){e[0] | (e[1] << 16), e[2] | (e[3] << 16), e[4] | (e[5] << 16), e[6] | (e[7] << 16)};
      } else {
        A[w][p] = *(const u32x4_u*)(kp + off);
      }
    }
  __syncthreads();
  f32x4 acc[8];
#pragma unroll
  for (int b = 0; b < 8; ++b) acc[b] = (f32x4){0, 0, 0, 0};
#pragma unroll
  for (int w = 0; w < 4; ++w) {
    u32x4 B[8];
#pragma unroll
    for (int b = 0; b < 8; ++b) B[b] = U[ch][30 + 32 * b + 2 * r + 1 - 4 * (4 * half + w) - h];
#pragma unroll
    for (int bp = 0; bp < 8; ++bp)
#pragma unroll
      for (int p = 0; p + bp < 8; ++p)
        acc[bp + p] = __builtin_amdgcn_mfma_f32_16x16x32_bf16(__builtin_bit_cast(bf16x8, A[w][p]),
                                                              __builtin_bit_cast(bf16x8, B[bp]), acc[bp + p], 0, 0, 0);
  }
  if (half) {
#pragma unroll
    for (int q = 0; q < 4; ++q) { R[wv][q][lane] = acc[q]; acc[q] = acc[4 + q]; }
  } else {
#pragma unroll
    for (int q = 0; q < 4; ++q) R[wv][q][lane] = acc[4 + q];
  }
  __syncthreads();
  const float Dv = bf2f(ssmD[d]);
#pragma unroll
  for (int q = 0; q < 4; ++q) {
    const f32x4 o = R[wv ^ 1][q][lane];
    const f32x4 y = acc[q] + o;
    const int b = 4 * half + q;
    const int s = 256 * b + 16 * r + 4 * h;
    const u32* up = (const u32*)&U[ch][30 + (s >> 3)];
    const int o2 = (s & 4) ? 0 : 2;
    const u32 w0 = up[o2], w1 = up[o2 + 1];
    const float z0 = rb(y[0]) + rb(hi2f(w1) * Dv);
    const float z1 = rb(y[1]) + rb(lo2f(w1) * Dv);
    const float z2 = rb(y[2]) + rb(hi2f(w0) * Dv);
    const float z3 = rb(y[3]) + rb(lo2f(w0) * Dv);
    *(u32x2*)(zT + (size_t)d * S + s) = (u32x2){pack2(z0, z1), pack2(z2, z3)};
  }
}

__device__ __forceinline__ void ln8(const float* x, float* o) {
  float s = 0;
#pragma unroll
  for (int e = 0; e < 8; ++e) s += x[e];
  const float mean = wave_sum(s) * (1.f / 512.f);
  float q = 0;
#pragma unroll
  for (int e = 0; e < 8; ++e) q += (x[e] - mean) * (x[e] - mean);
  const float var = rb(wave_sum(q) * (1.f / 512.f));
  const float m = rb(mean);
  const float sd = rb(sqrtf(rb(var + 1e-5f)));
#pragma unroll
  for (int e = 0; e < 8; ++e) o[e] = rb(rb(x[e] - m) / sd);
}

__device__ __forceinline__ void unpack8(u32x4 w, float* o) {
#pragma unroll
  for (int e = 0; e < 4; ++e) { o[2 * e] = lo2f(w[e]); o[2 * e + 1] = hi2f(w[e]); }
}

__global__ __launch_bounds__(512) void k_row(const u16* __restrict__ q, const u16* __restrict__ zT,
                                             const u16* __restrict__ mod, const u16* __restrict__ wr,
                                             u16* __restrict__ x1o, u16* __restrict__ h2o, float* __restrict__ ro,
                                             u16* __restrict__ out) {
  __shared__ u16 Z[8][512 + 8];
  const int tid = threadIdx.x, lane = tid & 63, wv = tid >> 6;
  const int s0 = blockIdx.x * 8;
  {
    const u32x4 z = *(const u32x4*)(zT + (size_t)tid * S + s0);
#pragma unroll
    for (int e = 0; e < 4; ++e) { Z[2 * e][tid] = (u16)(z[e] & 0xffff); Z[2 * e + 1][tid] = (u16)(z[e] >> 16); }
  }
  const int s = s0 + wv;
  float x[8], sa[8], ca[8], ga[8], sb[8], cb[8], gb[8], w[8], n[8], z[8];
  unpack8(*(const u32x4*)(q + (size_t)s * D + lane * 8), x);
  unpack8(*(const u32x4*)(mod + 0 * 512 + lane * 8), sa);
  unpack8(*(const u32x4*)(mod + 1 * 512 + lane * 8), ca);
  unpack8(*(const u32x4*)(mod + 2 * 512 + lane * 8), ga);
  unpack8(*(const u32x4*)(mod + 3 * 512 + lane * 8), sb);
  unpack8(*(const u32x4*)(mod + 4 * 512 + lane * 8), cb);
  unpack8(*(const u32x4*)(mod + 5 * 512 + lane * 8), gb);
  unpack8(*(const u32x4*)(wr + lane * 8), w);
  ln8(x, n);
  __syncthreads();
  unpack8(*(const u32x4*)(&Z[wv][lane * 8]), z);
  float x1[8], h2[8];
#pragma unroll
  for (int e = 0; e < 8; ++e) {
    const float h = rb(rb(n[e] * rb(1.f + ca[e])) + sa[e]);
    x1[e] = rb(x[e] + rb(ga[e] * rb(h * z[e])));
  }
  ln8(x1, n);
  float rs = 0;
  float o[8];
#pragma unroll
  for (int e = 0; e < 8; ++e) {
    h2[e] = rb(rb(n[e] * rb(1.f + cb[e])) + sb[e]);
    rs += h2[e] * w[e];
    o[e] = x1[e] + rb(gb[e] * h2[e]);
  }
  rs = rb(wave_sum(rs));
  if (lane == 0) ro[s] = rs;
  *(u32x4*)(x1o + (size_t)s * D + lane * 8) = (u32x4){pack2(x1[0], x1[1]), pack2(x1[2], x1[3]), pack2(x1[4], x1[5]), pack2(x1[6], x1[7])};
  *(u32x4*)(h2o + (size_t)s * D + lane * 8) = (u32x4){pack2(h2[0], h2[1]), pack2(h2[2], h2[3]), pack2(h2[4], h2[5]), pack2(h2[6], h2[7])};
  *(u32x4*)(out + (size_t)s * D + lane * 8) = (u32x4){pack2(o[0], o[1]), pack2(o[2], o[3]), pack2(o[4], o[5]), pack2(o[6], o[7])};
}

__device__ __forceinline__ u32 rkey(float r, int idx) {
  u32 b = __float_as_uint(r) >> 16;
  b = (b & 0x8000u) ? (~b & 0xffffu) : (b | 0x8000u);
  return (b << 16) | (u32)(0xffff - idx);
}

__device__ __forceinline__ void top8_wave(const float* ro, int lane, u32* sel  ) {
  u32 key[32];
#pragma unroll
  for (int j = 0; j < 8; ++j) {
    const f32x4 v = *(const f32x4*)(ro + j * 256 + lane * 4);
#pragma unroll
    for (int e = 0; e < 4; ++e) key[j * 4 + e] = rkey(v[e], j * 256 + lane * 4 + e);
  }
  u32 bound = 0xffffffffu;
  for (int it = 0; it < 8; ++it) {
    u32 m = 0;
#pragma unroll
    for (int j = 0; j < 32; ++j) m = max(m, key[j] < bound ? key[j] : 0u);
#pragma unroll
    for (int o = 32; o > 0; o >>= 1) m = max(m, (u32)__shfl_xor((int)m, o, 64));
    if (lane == 0) sel[it] = m;
    bound = m;
  }
}

__global__ __launch_bounds__(512) void k_ffn1(const float* __restrict__ ro, const u16* __restrict__ h2,
                                              const u16* __restrict__ wg, const u16* __restrict__ wu,
                                              u16* __restrict__ gu, u32* __restrict__ selo) {
  __shared__ u32 sel[8];
  __shared__ f32x4 R[8][2][64];
  const int tid = threadIdx.x, lane = tid & 63, wv = tid >> 6;
  const int n = lane & 15, h = lane >> 4;
  const int f0 = blockIdx.x * 16;
  u32x4 Bg[2], Bu[2];
#pragma unroll
  for (int ks = 0; ks < 2; ++ks) {
    u32 eg[8], eu[8];
#pragma unroll
    for (int j = 0; j < 8; ++j) {
      const int kk = wv * 64 + ks * 32 + 8 * h + j;
      eg[j] = wg[(size_t)kk * F + f0 + n];
      eu[j] = wu[(size_t)kk * F + f0 + n];
    }
    Bg[ks] = (u32x4){eg[0] | (eg[1] << 16), eg[2] | (eg[3] << 16), eg[4] | (eg[5] << 16), eg[6] | (eg[7] << 16)};
    Bu[ks] = (u32x4){eu[0] | (eu[1] << 16), eu[2] | (eu[3] << 16), eu[4] | (eu[5] << 16), eu[6] | (eu[7] << 16)};
  }
  if (wv == 0) top8_wave(ro, lane, sel);
  __syncthreads();
  if (blockIdx.x == 0 && tid < 8) selo[tid] = sel[tid];
  const int row = 0xffff - (sel[n & 7] & 0xffff);
  f32x4 ag = {0, 0, 0, 0}, au = {0, 0, 0, 0};
#pragma unroll
  for (int ks = 0; ks < 2; ++ks) {
    const u32x4 a = *(const u32x4*)(h2 + (size_t)row * D + wv * 64 + ks * 32 + 8 * h);
    ag = __builtin_amdgcn_mfma_f32_16x16x32_bf16(__builtin_bit_cast(bf16x8, a), __builtin_bit_cast(bf16x8, Bg[ks]), ag, 0, 0, 0);
    au = __builtin_amdgcn_mfma_f32_16x16x32_bf16(__builtin_bit_cast(bf16x8, a), __builtin_bit_cast(bf16x8, Bu[ks]), au, 0, 0, 0);
  }
  R[wv][0][lane] = ag;
  R[wv][1][lane] = au;
  __syncthreads();
  if (tid < 128) {
    const int tok = tid >> 4, col = tid & 15;
    const int l = (tok >> 2) * 16 + col, reg = tok & 3;
    float g = 0, u = 0;
#pragma unroll
    for (int w = 0; w < 8; ++w) { g += R[w][0][l][reg]; u += R[w][1][l][reg]; }
    g = rb(g); u = rb(u);
    const float sg = rb(g / (1.f + __expf(-g)));
    gu[tok * F + f0 + col] = (u16)f2bf(sg * u);
  }
}

__global__ __launch_bounds__(512) void k_ffn2(const u16* __restrict__ gu, const u16* __restrict__ wd,
                                              const u32* __restrict__ selo, const u16* __restrict__ x1,
                                              const u16* __restrict__ h2, const u16* __restrict__ mod,
                                              u16* __restrict__ out) {
  __shared__ f32x4 R[8][64];
  const int tid = threadIdx.x, lane = tid & 63, wv = tid >> 6;
  const int n = lane & 15, h = lane >> 4;
  const int d0 = blockIdx.x * 16;
  f32x4 acc = {0, 0, 0, 0};
#pragma unroll
  for (int ks = 0; ks < 7; ++ks) {
    u32 e[8];
    const int k0 = wv * 224 + ks * 32 + 8 * h;
#pragma unroll
    for (int j = 0; j < 8; ++j) e[j] = wd[(size_t)(k0 + j) * D + d0 + n];
    const u32x4 b = {e[0] | (e[1] << 16), e[2] | (e[3] << 16), e[4] | (e[5] << 16), e[6] | (e[7] << 16)};
    const u32x4 a = *(const u32x4*)(gu + (n & 7) * F + k0);
    acc = __builtin_amdgcn_mfma_f32_16x16x32_bf16(__builtin_bit_cast(bf16x8, a), __builtin_bit_cast(bf16x8, b), acc, 0, 0, 0);
  }
  R[wv][lane] = acc;
  __syncthreads();
  if (tid < 128) {
    const int tok = tid >> 4, col = tid & 15;
    const int l = (tok >> 2) * 16 + col, reg = tok & 3;
    float y = 0;
#pragma unroll
    for (int w = 0; w < 8; ++w) y += R[w][l][reg];
    const u32 key = selo[tok];
    const int row = 0xffff - (key & 0xffff);
    u32 rbits = key >> 16;
    rbits = (rbits & 0x8000u) ? (rbits & 0x7fffu) : (~rbits & 0xffffu);
    const float rv = bf2f(rbits);
    const float sig = rb(1.f / (1.f + __expf(-rv)));
    y = rb(rb(y) * sig);
    const int dd = d0 + col;
    const float hv = bf2f(h2[(size_t)row * D + dd]), xv = bf2f(x1[(size_t)row * D + dd]), gb = bf2f(mod[5 * 512 + dd]);
    out[(size_t)row * D + dd] = (u16)f2bf(xv + rb(gb * rb(hv + y)));
  }
}

extern "C" void kernel_launch(void* const* d_in, const int* in_sizes, int n_in, void* d_out, int out_size,
                              void* d_ws, size_t ws_size, hipStream_t stream) {
  const u16* q = (const u16*)d_in[0];
  const u16* k = (const u16*)d_in[1];
  const u16* v = (const u16*)d_in[2];
  const u16* shk = (const u16*)d_in[3];
  const u16* ssmk = (const u16*)d_in[4];
  const u16* ssmD = (const u16*)d_in[5];
  const u16* wr = (const u16*)d_in[7];
  const u16* wg = (const u16*)d_in[8];
  const u16* wu = (const u16*)d_in[9];
  const u16* wd = (const u16*)d_in[10];
  const u16* c = (const u16*)d_in[11];
  const u16* wmod = (const u16*)d_in[12];
  u16* out = (u16*)d_out;
  char* ws = (char*)d_ws;
  u16* kvT = (u16*)(ws);
  u16* zT = (u16*)(ws + (2 << 20));
  u16* x1 = (u16*)(ws + (4 << 20));
  u16* h2 = (u16*)(ws + (6 << 20));
  u16* mod = (u16*)(ws + (8 << 20));
  float* ro = (float*)(ws + (8 << 20) + 8192);
  u16* gu = (u16*)(ws + (8 << 20) + 32768);
  u32* selo = (u32*)(ws + (8 << 20) + 65536);
  k_prep<<<256 + 96, 256, 0, stream>>>(k, v, shk, c, wmod, kvT, mod);
  k_conv<<<256, 256, 0, stream>>>(kvT, ssmk, ssmD, zT);
  k_row<<<256, 512, 0, stream>>>(q, zT, mod, wr, x1, h2, ro, out);
  k_ffn1<<<112, 512, 0, stream>>>(ro, h2, wg, wu, gu, selo);
  k_ffn2<<<32, 512, 0, stream>>>(gu, wd, selo, x1, h2, mod, out);
}
```

```cpp
#include <hip/hip_runtime.h>
#include <stdint.h>

typedef unsigned short u16;
typedef unsigned int u32;
typedef unsigned long long u64;
typedef short bf16x8 __attribute__((ext_vector_type(8)));
typedef short s16x4 __attribute__((ext_vector_type(4)));
typedef float f32x4 __attribute__((ext_vector_type(4)));
typedef float f32x2 __attribute__((ext_vector_type(2)));
typedef __bf16 bf16x2 __attribute__((ext_vector_type(2)));
typedef u32 u32x4 __attribute__((ext_vector_type(4)));
typedef u32 u32x2 __attribute__((ext_vector_type(2)));
typedef __amdgpu_buffer_rsrc_t rsrc_t;
#define LAS __attribute__((address_space(3)))

constexpr int S = 2048, D = 512, F = 1792;
constexpr u32 SENT = 0xFFFFFFFFu;
constexpr int SC1 = 16, NT = 2;
constexpr int SPIN_CAP = 1 << 14;
#define RKS 32
#define JOBPOS 0
#define FW0 4
#define CHKSLP 4
#define ZSLP 0
#define MPOS 0
#define E1SLP 0
#define MRD 1
#define PFPOS 0
#define F1POS 0
#define WSEND 0
#define KSLP 0
#define MODSLP 0
#define F2S 8
#define SSMPOS 0
#define F1S 8
#define PF0SLP 0
#define GDELAY 200

constexpr int OFF_E1 = 0;
constexpr int OFF_E2 = 2 << 20;
constexpr int OFF_H2 = 4 << 20;
constexpr int OFF_SM = 6 << 20;
constexpr int OFF_MOD = OFF_SM + 524288;
constexpr int OFF_RK = 7 << 20;
constexpr int OFF_GU = OFF_SM + 65536;
constexpr int OFF_CK = OFF_SM + 262144;
constexpr int OFF_CZ = (7 << 20) + 524288;
constexpr int OFF_EP = OFF_SM + 131072;
constexpr int OFF_FL3 = OFF_SM + 196608;
constexpr int OFF_ST = 8 << 20;
constexpr int WS_BYTES = 16 << 20;
constexpr int OOB = 0x7ffffff0;

constexpr int KC_STRIDE = 4160;
constexpr int L_KC = 0;
constexpr int L_U = 33280;
constexpr int L_Z = L_U + 9216;
constexpr int L_W = L_Z + 8320;
constexpr int L_KV = L_W + 57344;
constexpr int L_KD = L_KV;
constexpr int L_SC = L_KV + 18432;
constexpr int L_MOD = L_SC + 1024;
constexpr int L_MP = L_MOD + 12288 + 32;
constexpr int L_GM = L_MP + 512;
constexpr int L_CNT = L_GM + 64;
constexpr int L_RANK = L_CNT + 64;
constexpr int L_SEL = L_RANK + 64;
constexpr int L_LIST = L_SEL + 32;
constexpr int L_END = L_LIST + 4096;

#define STAMP(i) do { } while (0)
#define FLUSH() do { } while (0)

template <int N> struct Int { static constexpr int value = N; };
#define PIN(x) asm volatile("" : "+v"(x))
#define PINS(x) asm volatile("" : "+s"(x))
#define PIN8(a) asm volatile("" : "+v"((a)[0]), "+v"((a)[1]), "+v"((a)[2]), "+v"((a)[3]), "+v"((a)[4]), "+v"((a)[5]), "+v"((a)[6]), "+v"((a)[7]))
#define FENCE() asm volatile("" ::: "memory")
#define RETRY(bad) if (__builtin_expect(bad, 0)) _Pragma("unroll 1") for (int spins = 0; spins < SPIN_CAP && (spins == 0 || (bad)); ++spins)
#define SCHED() __builtin_amdgcn_sched_barrier(0)
__device__ __forceinline__ float lo2f(u32 w) { return __uint_as_float(w << 16); }
__device__ __forceinline__ float hi2f(u32 w) { return __uint_as_float(w & 0xffff0000u); }
__device__ __forceinline__ f32x2 up2(u32 w) { return (f32x2){lo2f(w), hi2f(w)}; }
__device__ __forceinline__ u32 pk2(f32x2 v) { return __builtin_bit_cast(u32, __builtin_convertvector(v, bf16x2)); }
__device__ __forceinline__ u32 pk(float lo, float hi) { return pk2((f32x2){lo, hi}); }
__device__ __forceinline__ u32 nos(u32 x) { return min(x, SENT - 1u); }
__device__ __forceinline__ float rb(float x) { return (float)(__bf16)x; }
__device__ __forceinline__ f32x2 rb2(f32x2 v) { return up2(pk2(v)); }
__device__ __forceinline__ float mul1(float a, float b) { float r; asm("s_nop 0\n\tv_mul_f32 %0, %1, %2" : "=v"(r) : "v"(a), "v"(b)); return r; }
__device__ __forceinline__ float add1(float a, float b) { float r; asm("s_nop 0\n\tv_add_f32 %0, %1, %2" : "=v"(r) : "v"(a), "v"(b)); return r; }
__device__ __forceinline__ float mul0(float a, float b) { float r; asm("v_mul_f32 %0, %1, %2" : "=v"(r) : "v"(a), "v"(b)); return r; }
__device__ __forceinline__ float add0(float a, float b) { float r; asm("v_add_f32 %0, %1, %2" : "=v"(r) : "v"(a), "v"(b)); return r; }
__device__ __forceinline__ float fma0(float a, float b, float c) { float r; asm("v_fma_f32 %0, %1, %2, %3" : "=v"(r) : "v"(a), "v"(b), "v"(c)); return r; }
__device__ __forceinline__ u32 fold2(u32 a, u32 b) { return a + __builtin_amdgcn_alignbit(b, b, 21); }
__device__ __forceinline__ u32 umax4(u32x4 x) { return max(max(x.x, x.y), max(x.z, x.w)); }
__device__ __forceinline__ void bar() { asm volatile("s_waitcnt lgkmcnt(0)\n\ts_barrier" ::: "memory"); }
__device__ __forceinline__ void drain() { asm volatile("s_waitcnt vmcnt(0)" ::: "memory"); }
__device__ __forceinline__ rsrc_t mkr(const void* p, int bytes) { return __builtin_amdgcn_make_buffer_rsrc((void*)p, 0, bytes, 0x00020000); }
__device__ __forceinline__ u32x4 ld128(rsrc_t r, int off, int aux) {
  return aux == SC1 ? __builtin_amdgcn_raw_buffer_load_b128(r, off, 0, SC1)
                    : (aux == NT ? __builtin_amdgcn_raw_buffer_load_b128(r, off, 0, NT) : __builtin_amdgcn_raw_buffer_load_b128(r, off, 0, 0));
}
__device__ __forceinline__ void st128(u32x4 v, rsrc_t r, int off) { __builtin_amdgcn_raw_buffer_store_b128(v, r, off, 0, SC1); }
__device__ __forceinline__ f32x4 mfma(u32x4 a, u32x4 b, f32x4 c) {
  return __builtin_amdgcn_mfma_f32_16x16x32_bf16(__builtin_bit_cast(bf16x8, a), __builtin_bit_cast(bf16x8, b), c, 0, 0, 0);
}
__device__ __forceinline__ void dma16(rsrc_t r, int voff, int soff, u32 ldsaddr) {
  asm volatile("s_mov_b32 m0, %2\n\ts_nop 0\n\tbuffer_load_dwordx4 %0, %1, %3 offen nt lds" :: "v"(voff), "s"(r), "s"(ldsaddr), "s"(soff) : "memory");
}
__device__ __forceinline__ u32x2 trread(const char* p) {
  return __builtin_bit_cast(u32x2, __builtin_amdgcn_ds_read_tr16_b64_v4i16((LAS s16x4*)(LAS char*)p));
}
__device__ __forceinline__ float dot2(u32 a, u32 b, float c) {
  return __builtin_amdgcn_fdot2_f32_bf16(__builtin_bit_cast(bf16x2, a), __builtin_bit_cast(bf16x2, b), c, false);
}
__device__ __forceinline__ void dma16_sc1(rsrc_t r, int voff, int soff, u32 ldsaddr) {
  asm volatile("s_mov_b32 m0, %2\n\ts_nop 0\n\tbuffer_load_dwordx4 %0, %1, %3 offen sc1 lds" :: "v"(voff), "s"(r), "s"(ldsaddr), "s"(soff) : "memory");
}
__device__ __forceinline__ u32x4 trfrag(const char* p, int q4) {
  const u32x2 a = __builtin_bit_cast(u32x2, __builtin_amdgcn_ds_read_tr16_b64_v4i16((LAS s16x4*)(LAS char*)p));
  const u32x2 c = __builtin_bit_cast(u32x2, __builtin_amdgcn_ds_read_tr16_b64_v4i16((LAS s16x4*)(LAS char*)(p + q4)));
  return (u32x4){a.x, a.y, c.x, c.y};
}
template <int CTRL> __device__ __forceinline__ u32 dppu(u32 x) { return (u32)__builtin_amdgcn_mov_dpp((int)x, CTRL, 0xf, 0xf, true); }
template <int CTRL> __device__ __forceinline__ float dppf(float x) { return __uint_as_float(dppu<CTRL>(__float_as_uint(x))); }
constexpr int DPP_X1 = 0xB1, DPP_X2 = 0x4E, DPP_HM = 0x141, DPP_M = 0x140;
__device__ __forceinline__ float half_sum(float v) {
  v += dppf<DPP_X1>(v); v += dppf<DPP_X2>(v); v += dppf<DPP_HM>(v); v += dppf<DPP_M>(v);
  auto s = __builtin_amdgcn_permlane16_swap(__float_as_uint(v), __float_as_uint(v), false, false);
  return __uint_as_float(s[0]) + __uint_as_float(s[1]);
}
__device__ __forceinline__ void unpack16(u32x4 a, u32x4 c, f32x2* o) {
#pragma unroll
  for (int e = 0; e < 4; ++e) { o[e] = up2(a[e]); o[4 + e] = up2(c[e]); }
}
__device__ __forceinline__ u32 rank16(const char* lds, int lane, u32& mine) {
  mine = *(const u32*)(lds + (lane & 15) * 4);
  const u32x4 o = *(const u32x4*)(lds + (lane >> 4) * 16);
  u32 c = (o.x > mine) + (o.y > mine) + (o.z > mine) + (o.w > mine);
  auto s = __builtin_amdgcn_permlane16_swap(c, c, false, false);
  c = s[0] + s[1];
  auto t = __builtin_amdgcn_permlane32_swap(c, c, false, false);
  return t[0] + t[1];
}
__device__ __forceinline__ u32 arrive(u32 cnt, int idx) {
  u32 old, tmp;
  asm volatile("s_waitcnt lgkmcnt(0)\n\ts_mov_b64 exec, 1\n\tv_mov_b32 %1, 1\n\tds_add_rtn_u32 %1, %2, %1 offset:%3\n\ts_mov_b64 exec, -1\n\t"
               "s_waitcnt lgkmcnt(0)\n\tv_readfirstlane_b32 %0, %1" : "=s"(old), "=&v"(tmp) : "v"(cnt), "i"(idx * 4) : "memory");
  return old;
}
__device__ __forceinline__ void signal(u32 cnt, int idx) {
  u32 tmp;
  asm volatile("s_waitcnt lgkmcnt(0)\n\ts_mov_b64 exec, 1\n\tv_mov_b32 %0, 1\n\tds_add_u32 %1, %0 offset:%2\n\ts_mov_b64 exec, -1"
               : "=&v"(tmp) : "v"(cnt), "i"(idx * 4) : "memory");
}
__device__ __forceinline__ u32 peek(u32 cnt, int idx) {
  u32 v, tmp;
  asm volatile("ds_read_b32 %1, %2 offset:%3\n\ts_waitcnt lgkmcnt(0)\n\tv_readfirstlane_b32 %0, %1" : "=s"(v), "=&v"(tmp) : "v"(cnt), "i"(idx * 4) : "memory");
  return v;
}
template <int SLEEP> __device__ __forceinline__ void await(u32 cnt, int idx, u32 n) {
#pragma unroll 1
  for (int spins = 0; peek(cnt, idx) < n && spins < (SPIN_CAP << 6); ++spins) if (SLEEP) __builtin_amdgcn_s_sleep(SLEEP);
}

typedef u32 u32x16 __attribute__((ext_vector_type(16)));
typedef u64 u64x8 __attribute__((ext_vector_type(8)));
typedef u64 u64x2 __attribute__((ext_vector_type(2)));
constexpr u64 desc_hi(u32 bytes) { return (u64)bytes | ((u64)0x00020000u << 32); }
#define SUB4(v, i) __builtin_shufflevector(v, v, 4 * (i), 4 * (i) + 1, 4 * (i) + 2, 4 * (i) + 3)

__global__ __launch_bounds__(512, 2) void fused_kernel(
    char* a0, u64 a1, const u16* a2, u64 a3, const u16* a4, u64 a5, const u16* a6, u64 a7, const u16* a8, const u16* a9,
    const u16* a10, const u16* a11, const u16* a12, const u16* a13, const u16* a14, const u16* a15, const u16* a16, u16* a17) {
  __shared__ __attribute__((aligned(16))) char lds[L_END];
  u32x16 ka0; u64x8 ka1; u64x2 ka2;
  asm volatile("s_load_dwordx16 %0, %3, 0x0\n\ts_load_dwordx16 %1, %3, 0x40\n\ts_load_dwordx4 %2, %3, 0x80"
               : "=&s"(ka0), "=&s"(ka1), "=&s"(ka2) : "s"(__builtin_amdgcn_kernarg_segment_ptr()));
  SCHED();
  STAMP(0);
  const int tid = threadIdx.x, lane = tid & 63;
  const int w = __builtin_amdgcn_readfirstlane(tid >> 6);
  const int b = blockIdx.x, g = b & 7, m = b >> 3, j = 32 * g + m, cg = (g + 1) & 7;
  u32x4 s4 = {SENT, SENT, SENT, SENT};

  const u32 ldsb = (u32)(size_t)(LAS char*)lds;
  int kvo = ((tid >> 3) << 10) | ((((tid & 7) ^ ((tid >> 3) & 2) ^ ((tid >> 4) & 4)) ^ 4) << 4);
  u32 cls = OFF_E1 + cg * 262144 + m * 256, kvs = m * 65536 + cg * 128, kml = ldsb + L_KV + 1024 + w * 1024, vml = kml + 8192;
  int kho = (lane >= 40 && m > 0) ? ((kvo & 0x1fff) ^ 64) : OOB;
  int sko = lane < 32 ? lane * 16 : OOB;
  asm volatile("" : "+v"(kvo), "+v"(kho), "+v"(sko));
  asm volatile("" : "+s"(kvs), "+s"(kml), "+s"(vml));
  SCHED();
  asm volatile("s_waitcnt lgkmcnt(0)" : "+s"(ka0), "+s"(ka1), "+s"(ka2));
  asm volatile("s_mov_b32 m0, %3\n\ts_nop 0\n\tbuffer_load_dwordx4 %0, %1, %2 offen nt lds\n\t"
               "s_mov_b32 m0, %5\n\ts_nop 0\n\tbuffer_load_dwordx4 %0, %4, %2 offen nt lds"
               :: "v"(kvo), "s"(SUB4(ka0, 1)), "s"(kvs), "s"(kml), "s"(SUB4(ka0, 2)), "s"(vml) : "memory");
  if (w == 4)
    asm volatile("s_mov_b32 m0, %3\n\ts_nop 0\n\tbuffer_load_dwordx4 %0, %1, %2 offen nt lds"
                 :: "v"(kho), "s"(SUB4(ka0, 1)), "s"(m > 0 ? kvs - 8192 : 0u), "s"(ldsb + L_KV) : "memory");
  if (w == 5)
    asm volatile("s_mov_b32 m0, %3\n\ts_nop 0\n\tbuffer_load_dwordx4 %0, %1, %2 offen lds"
                 :: "v"(sko), "s"(SUB4(ka0, 3)), "s"(cg * 512), "s"(ldsb + L_KV + 17408) : "memory");
  SCHED();
  char* ws = (char*)(((u64)ka0[1] << 32) | ka0[0]);
  const u16 *gssmk = (const u16*)ka1[0], *gc = (const u16*)ka1[1], *gwmod = (const u16*)ka1[2], *gq = (const u16*)ka1[3],
            *gssmD = (const u16*)ka1[4], *gwr = (const u16*)ka1[5], *gwg = (const u16*)ka1[6], *gwu = (const u16*)ka1[7], *gwd = (const u16*)ka2[0];
  u16* gout = (u16*)ka2[1];
  const rsrc_t rW = mkr(ws, WS_BYTES);
  const bool jobG = g == 7, jobF = g < 7 && m >= 16, jobM = g < 6 && m < 16;
  const int mcol = g * 512 + m * 32;
  int e1_out = cls + ((w & 3) * 8 + ((lane & 15) >> 1)) * 8192 + (w >> 2) * 128 + (lane & 1) * 64 + (lane >> 4) * 16;
  STAMP(16);
  if (w >= 6) {
    const int at = tid - 384;
    int zo = L_CNT + (at & 3) * 16;
    if (at < 60) zo = L_U + (at >= 30 ? 4608 - 480 : 0) + at * 16;
    if (at >= 64 && at < 80) zo = L_KC + ((at >> 1) & 7) * KC_STRIDE + (at & 1) * 16;
    if (at >= 80 && at < 84) zo = L_LIST + (at & 3) * 16;
    *(u32x4*)(lds + zo) = (u32x4){0, 0, 0, 0};
  }
  const int f0 = g * 256 + (m - 16) * 16;
  const int gd0 = m * 16;
  u32 cnt = ldsb + L_CNT;
  PIN(cnt);
  u64 ep;
#define EPOCH() asm volatile("s_load_dwordx2 %0, %1, %2\n\ts_waitcnt lgkmcnt(0)" : "=&s"(ep) : "s"(ws), "s"(OFF_EP) : "memory"); STAMP(18)
  u32x2 tagv;
#define MAKE_TAG() do { const u64 tag = ep + 1ull; tagv = (u32x2){(u32)tag, (u32)(tag >> 32)}; } while (0)
  auto publish_kv = [&](auto younger, auto hook) __attribute__((always_inline)) {
    const int blk = 4 * (w >> 2) + (lane >> 4) + 1;
    int ko = (blk * 8 + ((lane & 15) >> 2)) * 128 + (((w & 3) ^ ((lane >> 3) & 1) ^ ((blk & 1) << 1)) << 5) + (lane & 3) * 8;
    int cko = (lane & 17) == 0 ? OFF_CK + ((cg * 4 + (w & 3)) * 64 + m * 2 + (w >> 2)) * 64 + (lane & 14) * 4 + (lane >> 5) * 4 : OOB;
    u32 tw = lane < 32 ? tagv.x : tagv.y;
    PIN(ko); PIN(e1_out); PIN(cko); PIN(tw);
    int sko2 = L_KV + 17408 + ((w & 3) * 16 + (lane & 15)) * 8, ko2 = L_KV - 512 + (ko ^ 64);
    PIN(sko2); PIN(ko2);
    asm volatile("s_waitcnt vmcnt(%0)\n\ts_barrier" :: "i"(younger.value) : "memory");
    STAMP(20);
    hook();
    const char* kp = lds + L_KV + ko;
    const u32x2 sk = *(const u32x2*)(lds + sko2);
    const u32x2 p0 = trread(lds + ko2), p1 = trread(kp), p2 = trread(kp + 512), v0 = trread(kp + 8192), v1 = trread(kp + 8704);
    const u32 c10 = __builtin_amdgcn_alignbyte(sk.x, sk.x, 2), c32 = __builtin_amdgcn_alignbyte(sk.y, sk.y, 2);
    const u32 c0z = sk.x & 0xffffu, cz3 = sk.y & 0xffff0000u, c21 = (sk.y & 0xffffu) | (sk.x & 0xffff0000u);
    const u32 P[6] = {p0.x, p0.y, p1.x, p1.y, p2.x, p2.y}, V[4] = {v0.x, v0.y, v1.x, v1.y};
    u32 o[4];
#pragma unroll
    for (int i = 0; i < 4; ++i) {
      const float ev = dot2(P[i + 2], c0z, dot2(P[i + 1], c21, dot2(P[i], cz3, 0.f)));
      const float od = dot2(P[i + 2], c10, dot2(P[i + 1], c32, 0.f));
      o[i] = pk(od * hi2f(V[i]), ev * lo2f(V[i]));
    }
    st128((u32x4){o[3], o[2], o[1], o[0]}, rW, e1_out);
    FENCE();
    {
      auto t = __builtin_amdgcn_permlane32_swap(fold2(o[3], o[2]), fold2(o[1], o[0]), false, false);
      u32 hs = t[0] + t[1];
      auto u = __builtin_amdgcn_permlane16_swap(hs, hs, false, false);
      hs = u[0] + u[1];
      hs += dppu<DPP_X1>(hs);
      __builtin_amdgcn_raw_buffer_store_b32(hs + tw, rW, cko, 0, SC1);
    }
    FENCE();
    STAMP(2);
  };
  const rsrc_t rO = mkr(gout, S * D * 2);

  if (w < 4) {
    const int ch = w >> 1, half = w & 1, r = lane & 15, h = lane >> 4;
    u32x2 kx;
    auto taps = [&]() __attribute__((always_inline)) {
      const rsrc_t rS = mkr(gssmk, S * D * 2);
      const u32 dst = ldsb + L_KC + ch * (4 * KC_STRIDE) + 32 + half * 2048;
      dma16(rS, lane * 16, j * 8192 + w * 2048, dst);
      dma16(rS, lane * 16, j * 8192 + w * 2048 + 1024, dst + 1024);
      kx = __builtin_amdgcn_raw_buffer_load_b64(rS, j * 8192 + w * 2048 + 2048, 0, NT);
      FENCE();
    };
    if (SSMPOS == 0) taps();
    EPOCH();
    MAKE_TAG();
    int cki = OFF_CK + ((g * 4 + (m >> 3)) * 64 + w * 8 + (lane >> 3)) * 64 + (m & 7) * 8, e1i = OFF_E1 + j * 8192 + tid * 16;
    int o_z = OFF_E2 + j * 8192 + (128 * w + 4 * r + h) * 16;
    int o_cz = OFF_CZ + j * 2048 + w * 512 + r * 16 + h * 4;
    u32 tw2 = (h & 1) ? tagv.y : tagv.x;
    PIN(cki); PIN(e1i); PIN(o_z); PIN(o_cz); PIN(tw2);
    publish_kv(Int<SSMPOS == 0 ? 3 : 0>{}, []() {});
    if (MPOS == 0 && jobM) signal(cnt, 12);
    if (SSMPOS == 1) taps();
    asm volatile("" : "+v"(kx) :: "memory");
    STAMP(21);
    {
      char* kc = lds + L_KC + ch * (4 * KC_STRIDE) + (half * 128 + lane + 2) * 16;
      u32x4 kd[2];
      u32x2 kn[2];
#pragma unroll
      for (int i = 0; i < 2; ++i) { kd[i] = *(const u32x4*)(kc + i * 1024); kn[i] = *(const u32x2*)(kc + i * 1024 + 16); }
      if (lane == 63) kn[1] = kx;
#pragma unroll
      for (int i = 0; i < 2; ++i) {
        const u32 a01 = __builtin_amdgcn_alignbyte(kd[i].y, kd[i].x, 2), a12 = __builtin_amdgcn_alignbyte(kd[i].z, kd[i].y, 2);
        const u32 a23 = __builtin_amdgcn_alignbyte(kd[i].w, kd[i].z, 2), a34 = __builtin_amdgcn_alignbyte(kn[i].x, kd[i].w, 2);
        const u32 a45 = __builtin_amdgcn_alignbyte(kn[i].y, kn[i].x, 2);
        *(u32x4*)(kc + i * 1024 + KC_STRIDE) = (u32x4){a01, a12, a23, a34};
        *(u32x4*)(kc + i * 1024 + 2 * KC_STRIDE) = (u32x4){kd[i].y, kd[i].z, kd[i].w, kn[i].x};
        *(u32x4*)(kc + i * 1024 + 3 * KC_STRIDE) = (u32x4){a12, a23, a34, a45};
        if (i == 0 && half == 0 && lane == 0) {
          *(u32*)(kc - 4 + KC_STRIDE) = kd[0].x << 16;
          *(u32*)(kc - 4 + 2 * KC_STRIDE) = kd[0].x;
          *(u32x2*)(kc - 8 + 3 * KC_STRIDE) = (u32x2){kd[0].x << 16, a01};
        }
      }
    }
    STAMP(22);
    if (ch == 0) { if (arrive(cnt, 6) != 1) await<0>(cnt, 6, 2); } else { if (arrive(cnt, 7) != 1) await<0>(cnt, 7, 2); }
    STAMP(3);
    if (E1SLP < 0) drain(); else if (E1SLP) __builtin_amdgcn_s_sleep(E1SLP);
    STAMP(4);
    u32x4 x0 = ld128(rW, e1i, SC1), x1 = ld128(rW, e1i + 4096, SC1);
    u32x2 c0 = __builtin_amdgcn_raw_buffer_load_b64(rW, cki, 0, SC1), c1 = __builtin_amdgcn_raw_buffer_load_b64(rW, cki + 2048, 0, SC1);
    FENCE();
    if (MPOS == 2 && jobM) signal(cnt, 12);
    u32x2 Al[32], Ah[32];
    {
      const u32 ka = ldsb + L_KC + ch * (4 * KC_STRIDE) + ((1 + r) & 3) * KC_STRIDE + 2 * (128 * half + 8 * h + ((1 + r) & ~3));
#define RD4(i) asm volatile("ds_read_b64 %0, %8 offset:%9\n\tds_read_b64 %1, %8 offset:%9+8\n\tds_read_b64 %2, %8 offset:%9+64\n\t" \
      "ds_read_b64 %3, %8 offset:%9+72\n\tds_read_b64 %4, %8 offset:%9+128\n\tds_read_b64 %5, %8 offset:%9+136\n\t" \
      "ds_read_b64 %6, %8 offset:%9+192\n\tds_read_b64 %7, %8 offset:%9+200" \
      : "=&v"(Al[i]), "=&v"(Ah[i]), "=&v"(Al[8 + i]), "=&v"(Ah[8 + i]), "=&v"(Al[16 + i]), "=&v"(Ah[16 + i]), "=&v"(Al[24 + i]), "=&v"(Ah[24 + i]) \
      : "v"(ka), "i"(512 * i) : "memory")
      RD4(0); RD4(1); RD4(2); RD4(3); RD4(4); RD4(5); RD4(6); RD4(7);
      asm volatile("s_waitcnt lgkmcnt(0)" ::: "memory");
      PIN8(Al); PIN8(Al + 8); PIN8(Al + 16); PIN8(Al + 24); PIN8(Ah); PIN8(Ah + 8); PIN8(Ah + 16); PIN8(Ah + 24);
    }
    STAMP(5);
    {
      auto bad = [&]() __attribute__((always_inline)) {
        u32 h[4] = {fold2(x0.x, x0.y), fold2(x0.z, x0.w), fold2(x1.x, x1.y), fold2(x1.z, x1.w)};
#pragma unroll
        for (int i = 0; i < 4; ++i) h[i] += dppu<DPP_X1>(h[i]);
#pragma unroll
        for (int i = 0; i < 4; ++i) h[i] += dppu<DPP_X2>(h[i]);
#pragma unroll
        for (int i = 0; i < 4; ++i) h[i] += dppu<DPP_HM>(h[i]);
        return (((h[0] + tagv.x) ^ c0.x) | ((h[1] + tagv.y) ^ c0.y) | ((h[2] + tagv.x) ^ c1.x) | ((h[3] + tagv.y) ^ c1.y)) != 0;
      };
      bool nb = bad();
      RETRY(__any(nb)) {
        FENCE();
        if (nb) {
          x0 = ld128(rW, e1i, SC1); x1 = ld128(rW, e1i + 4096, SC1);
          c0 = __builtin_amdgcn_raw_buffer_load_b64(rW, cki, 0, SC1); c1 = __builtin_amdgcn_raw_buffer_load_b64(rW, cki + 2048, 0, SC1);
        }
        asm volatile("" : "+v"(x0), "+v"(x1), "+v"(c0), "+v"(c1));
        nb = bad();
      }
      STAMP(1);
      char* u0 = lds + L_U + ((tid >> 2) & 1) * 4608 + (30 + (tid >> 3) * 4 + (tid & 3)) * 16;
      *(u32x4*)(u0) = x0;
      *(u32x4*)(u0 + 2048) = x1;
    }
    if (arrive(cnt, 3) != 3) await<0>(cnt, 3, 4);
    STAMP(6);
    {
      f32x4 acc[8];
#pragma unroll
      for (int t = 0; t < 8; ++t) acc[t] = (f32x4){0, 0, 0, 0};
      const char* ub = lds + L_U + ch * 4608 + (30 + 2 * r + 1 - 16 * half - h) * 16;
      char* rp = lds + L_KC + w * 8192 + lane * 16;
      u32x4 B0[8], B1[8];
#pragma unroll
      for (int t = 0; t < 8; ++t) B0[t] = *(const u32x4*)(ub + 512 * t);
#pragma unroll
      for (int t = 0; t < 8; ++t) B1[t] = *(const u32x4*)(ub + 512 * t - 64);
      SCHED();
#define AF(ww, p) (u32x4){Al[8 * (ww) + (p)].x, Al[8 * (ww) + (p)].y, Ah[8 * (ww) + (p)].x, Ah[8 * (ww) + (p)].y}
#pragma unroll
      for (int bp = 0; bp < 8; ++bp) {
#pragma unroll
        for (int t = 7; t >= bp; --t) acc[t] = mfma(AF(0, t - bp), B0[bp], acc[t]);
        SCHED();
      }
#pragma unroll
      for (int bp = 0; bp < 8; ++bp) {
        B0[bp] = *(const u32x4*)(ub + 512 * bp - 128);
#pragma unroll
        for (int t = 7; t >= bp; --t) acc[t] = mfma(AF(1, t - bp), B1[bp], acc[t]);
        SCHED();
      }
#pragma unroll
      for (int bp = 0; bp < 8; ++bp) {
        B1[bp] = *(const u32x4*)(ub + 512 * bp - 192);
#pragma unroll
        for (int t = 7; t >= bp; --t) acc[t] = mfma(AF(2, t - bp), B0[bp], acc[t]);
        SCHED();
      }
#pragma unroll
      for (int t = 0; t < 8; ++t) {
#pragma unroll
        for (int bp = 0; bp <= t; ++bp) {
          acc[t] = mfma(AF(3, t - bp), B1[bp], acc[t]);
          if (t >= 2 && bp == 1) { SCHED(); *(f32x4*)(rp + (t - 1) * 1024) = acc[t - 1]; if (t == 2) *(f32x4*)(rp) = acc[0]; SCHED(); }
        }
        SCHED();
      }
      *(f32x4*)(rp + 7 * 1024) = acc[7];
    }
    STAMP(7);
    if (arrive(cnt, 14) != 7) await<0>(cnt, 14, 8);
    u32x4 zt[2];
#pragma unroll
    for (int t = 0; t < 2; ++t) {
      const char* rp = lds + L_KC + (2 * w + t) * 1024 + lane * 16;
      const char* dp = lds + L_KD + (2 * w + t) * 1024 + lane * 16;
      const f32x4 ya = *(const f32x4*)(rp) + *(const f32x4*)(rp + 8192) + *(const f32x4*)(dp);
      const f32x4 yb = *(const f32x4*)(rp + 16384) + *(const f32x4*)(rp + 24576) + *(const f32x4*)(dp + 8192);
      zt[t] = (u32x4){pk(ya[0], yb[0]), pk(ya[1], yb[1]), pk(ya[2], yb[2]), pk(ya[3], yb[3])};
      st128(zt[t], rW, o_z + 1024 * t);
    }
    FENCE();
#pragma unroll
    for (int t = 0; t < 2; ++t) {
      auto u = __builtin_amdgcn_permlane16_swap(fold2(zt[t].x, zt[t].y), fold2(zt[t].z, zt[t].w), false, false);
      __builtin_amdgcn_raw_buffer_store_b32(u[0] + u[1] + tw2, rW, o_cz + 256 * t, 0, SC1);
    }
    signal(cnt, 9);
    STAMP(15);
    u32x4 Wf[14];
    const int wvo = jobG ? (w * 448 + (lane >> 1)) * 1024 + gd0 * 2 + (lane & 1) * 16 : (w * 128 + (lane >> 1)) * (F * 2) + f0 * 2 + (lane & 1) * 16;
    if (F1POS == 0 && jobF) {
#pragma unroll
      for (int i = 0; i < 4; ++i) dma16(mkr(gwg, D * F * 2), wvo, i * 32 * F * 2, ldsb + L_W + w * 4096 + i * 1024);
      asm volatile("s_waitcnt vmcnt(4)" ::: "memory");
    } else if (jobG) {
#pragma unroll
      for (int i = 0; i < 4; ++i) dma16(mkr(gwd, F * D * 2), wvo, i * 32 * 1024, ldsb + L_W + w * 14336 + i * 1024);
      asm volatile("s_waitcnt vmcnt(4)" ::: "memory");
    } else {
      drain();
    }
    STAMP(8);
    {
      const int zo = OFF_E2 + (tid >> 1) * 8192 + j * 32 + (tid & 1) * 16, zc = OFF_CZ + (tid >> 1) * 2048 + j * 8 + (tid & 1) * 4;
      u32x4 z0 = ld128(rW, zo, SC1), z1 = ld128(rW, zo + 128 * 8192, SC1);
      u32 k0 = __builtin_amdgcn_raw_buffer_load_b32(rW, zc, 0, SC1), k1 = __builtin_amdgcn_raw_buffer_load_b32(rW, zc + 128 * 2048, 0, SC1);
      const u32 tw3 = (lane & 1) ? tagv.y : tagv.x;
      auto bad = [&]() __attribute__((always_inline)) {
        const u32 a0 = fold2(z0.x, z0.y), a1 = fold2(z0.z, z0.w), b0 = fold2(z1.x, z1.y), b1 = fold2(z1.z, z1.w);
        const u32 sa0 = a0 + dppu<DPP_X1>(a0), sa1 = a1 + dppu<DPP_X1>(a1), sb0 = b0 + dppu<DPP_X1>(b0), sb1 = b1 + dppu<DPP_X1>(b1);
        u32 d = ((((lane & 1) ? sa1 : sa0) + tw3) ^ k0) | ((((lane & 1) ? sb1 : sb0) + tw3) ^ k1);
        return d;
      };
      u32 nb = bad();
      STAMP(17);
      RETRY(__any(nb != 0)) {
        FENCE();
        nb |= dppu<DPP_X1>(nb);
        if (nb) {
          z0 = ld128(rW, zo, SC1); z1 = ld128(rW, zo + 128 * 8192, SC1);
          k0 = __builtin_amdgcn_raw_buffer_load_b32(rW, zc, 0, SC1); k1 = __builtin_amdgcn_raw_buffer_load_b32(rW, zc + 128 * 2048, 0, SC1);
        }
        asm volatile("" : "+v"(z0), "+v"(z1), "+v"(k0), "+v"(k1));
        nb = bad();
      }
      STAMP(9);
      char* zp = lds + L_Z + (tid & 1) * 4160 + (tid >> 1) * 4;
#pragma unroll
      for (int e = 0; e < 4; ++e) { *(u32*)(zp + e * 1040) = z0[e]; *(u32*)(zp + e * 1040 + 512) = z1[e]; }
    }
    bar();
    if (!(jobF || jobG)) { FLUSH(); return; }
    await<2>(cnt, 10, 4);
    if (jobG) {
#pragma unroll
      for (int i = 4; i < 8; ++i) dma16(mkr(gwd, F * D * 2), wvo, i * 32 * 1024, ldsb + L_W + w * 14336 + i * 1024);
    } else {
#pragma unroll
      for (int i = 0; i < 4; ++i) dma16(mkr(gwu, D * F * 2), wvo, i * 32 * F * 2, ldsb + L_W + 16384 + w * 4096 + i * 1024);
    }
    bar();
    if (KSLP) __builtin_amdgcn_s_sleep(KSLP);
    STAMP(11);
    const u64 tb6 = __builtin_amdgcn_s_memrealtime();
    const int ko = OFF_RK + g * 256 * RKS + (tid >> 1) * RKS + (tid & 1) * 16;
    u32x4 ka = ld128(rW, ko, SC1), kb = ld128(rW, ko + 128 * RKS, SC1);
    if (jobF) {
      asm volatile("s_waitcnt vmcnt(2)" ::: "memory");
      const char* wp = lds + L_W + w * 4096 + h * 256 + r * 8;
#pragma unroll
      for (int i = 0; i < 8; ++i) Wf[i] = trfrag(wp + (i >> 2) * 16384 + (i & 3) * 1024, 128);
      PIN8(Wf);
    }
    asm volatile("" : "+v"(ka), "+v"(kb) :: "memory");
    u32 mx = max(umax4(ka), umax4(kb));
    RETRY(!__all(mx != SENT)) {
      FENCE();
      if (mx == SENT) { ka = ld128(rW, ko, SC1); kb = ld128(rW, ko + 128 * RKS, SC1); mx = max(umax4(ka), umax4(kb)); }
    }
    if (jobG) {
#pragma unroll
      for (int i = 8; i < 14; ++i) dma16(mkr(gwd, F * D * 2), wvo, i * 32 * 1024, ldsb + L_W + w * 14336 + i * 1024);
    }
    STAMP(12);
    u32 gm = max(mx, dppu<DPP_X1>(mx));
    gm = max(gm, dppu<DPP_X2>(gm)); gm = max(gm, dppu<DPP_HM>(gm)); gm = max(gm, dppu<DPP_M>(gm));
    if (r == 0) *(u32*)(lds + L_GM + (4 * w + h) * 4) = gm;
    const u32 ky[8] = {ka.x, ka.y, ka.z, ka.w, kb.x, kb.y, kb.z, kb.w};
    u32 m2 = 0;
#pragma unroll
    for (int e = 0; e < 8; ++e) m2 = max(m2, ky[e] == mx ? 0u : ky[e]);
    PIN(m2);
    bar();
    u32 mine;
    u32 rk = rank16(lds + L_GM, lane, mine);
    const u32 T = __builtin_amdgcn_readlane(mine, __builtin_ctzll(__ballot(rk == 7)));
    if (mx >= T) {
      const u32 pos = __hip_atomic_fetch_add((u32*)(lds + L_CNT), 1u, __ATOMIC_RELAXED, __HIP_MEMORY_SCOPE_WORKGROUP);
      *(u32*)(lds + L_LIST + pos * 4) = mx;
    }
    if (__any(m2 >= T)) {
#pragma unroll
      for (int e = 0; e < 8; ++e)
        if (ky[e] >= T && ky[e] != mx) {
          const u32 pos = __hip_atomic_fetch_add((u32*)(lds + L_CNT), 1u, __ATOMIC_RELAXED, __HIP_MEMORY_SCOPE_WORKGROUP);
          *(u32*)(lds + L_LIST + pos * 4) = ky[e];
        }
    }
    bar();
    u32 cand = *(const u32*)(lds + L_LIST + r * 4);
    const u32 N = peek(cnt, 0);
    if (N > 16) {
      for (u32 i = tid; i < N; i += 256) {
        const u32 kx = *(const u32*)(lds + L_LIST + i * 4);
        u32 c = 0;
#pragma unroll 1
        for (u32 q = 0; q < N; ++q) c += *(const u32*)(lds + L_LIST + q * 4) > kx;
        if (c < 8) *(u32*)(lds + L_SEL + c * 4) = kx;
      }
      if (arrive(cnt, 15) != 3) await<0>(cnt, 15, 4);
      if (tid < 16) *(u32*)(lds + L_LIST + tid * 4) = tid < 8 ? *(const u32*)(lds + L_SEL + tid * 4) : 0u;
      if (arrive(cnt, 15) != 7) await<0>(cnt, 15, 8);
      cand = *(const u32*)(lds + L_LIST + r * 4);
    }
    STAMP(13);
    if (jobF) {
      const int ho = OFF_H2 + (2047 - (int)(cand & 0x7ffu)) * 1024 + (w * 128 + 8 * h) * 2;
      u32x4 a[4];
#pragma unroll
      for (int ks = 0; ks < 4; ++ks) a[ks] = ld128(rW, ho + ks * 64, SC1);
      FENCE();
      rk = rank16(lds + L_LIST, lane, mine);
      if (w == 0 && lane < 16 && rk < 8) *(u32*)(lds + L_RANK + rk * 4) = (lane >> 2) * 256 + (lane & 3) * 4;
      RETRY(!__all(max(max(umax4(a[0]), umax4(a[1])), max(umax4(a[2]), umax4(a[3]))) != SENT)) {
        FENCE();
#pragma unroll
        for (int ks = 0; ks < 4; ++ks) a[ks] = ld128(rW, ho + ks * 64, SC1);
        PIN(a[0]); PIN(a[1]); PIN(a[2]); PIN(a[3]);
      }
      f32x4 ag = {0, 0, 0, 0}, au = {0, 0, 0, 0};
#pragma unroll
      for (int ks = 0; ks < 4; ++ks) { ag = mfma(a[ks], Wf[ks], ag); au = mfma(a[ks], Wf[4 + ks], au); }
      *(f32x4*)(lds + L_KC + w * 2048 + lane * 16) = ag;
      *(f32x4*)(lds + L_KC + w * 2048 + 1024 + lane * 16) = au;
      bar();
      STAMP(14);
      if (w < 2) {
        const char* p = lds + L_KC + *(const u32*)(lds + L_RANK + (lane >> 3) * 4) + (8 * w + (lane & 7)) * 16;
        float gv = 0, uv = 0;
#pragma unroll
        for (int i = 0; i < 4; ++i) { gv += *(const float*)(p + i * 2048); uv += *(const float*)(p + i * 2048 + 1024); }
        gv = rb(gv);
        const float v = rb(gv * __builtin_amdgcn_rcpf(1.f + __expf(-gv))) * rb(uv);
        const float vn = dppf<DPP_X1>(v);
        __builtin_amdgcn_raw_buffer_store_b32(nos(pk(v, vn)), rW, (lane & 1) ? OOB : OFF_GU + f0 * 16 + w * 128 + lane * 2, 0, SC1);
        STAMP(10);
      }
    } else {
      rk = rank16(lds + L_LIST, lane, mine);
      if (w == 0 && lane < 16 && rk < 8) *(u32*)(lds + L_SEL + rk * 4) = mine;
      bar();
      drain();
      {
        const char* wp = lds + L_W + w * 14336 + h * 256 + r * 8;
#pragma unroll
        for (int i = 0; i < 14; ++i) Wf[i] = trfrag(wp + i * 1024, 128);
        PIN8(Wf); PIN8(Wf + 6);
      }
      while (__builtin_amdgcn_s_memrealtime() < tb6 + GDELAY) __builtin_amdgcn_s_sleep(1);
      STAMP(14);
      const int go = OFF_GU + (w * 56 + 4 * (r >> 3) + h) * 128 + (r & 7) * 16;
      u32x4 a[7];
#pragma unroll 1
      for (int spins = 0; spins < SPIN_CAP; ++spins) {
        FENCE();
        u32 mm = 0;
#pragma unroll
        for (int i = 0; i < 7; ++i) a[i] = ld128(rW, go + i * 1024, SC1);
#pragma unroll
        for (int i = 0; i < 7; ++i) mm = max(mm, umax4(a[i]));
        if (__all(mm != SENT)) break;
      }
      STAMP(10);
      f32x4 a0 = {0, 0, 0, 0}, a1 = {0, 0, 0, 0};
#pragma unroll
      for (int i = 0; i < 7; ++i) { a0 = mfma(a[i], Wf[2 * i], a0); a1 = mfma(a[i], Wf[2 * i + 1], a1); }
      *(f32x4*)(lds + L_KC + w * 1024 + lane * 16) = lane < 32 ? a0 : a1;
      bar();
    }
    FLUSH();
  } else {
    const int a = w - 4, at = tid - 256, hl = lane & 31, row = 8 * j + 2 * a + (lane >> 5);
    __builtin_amdgcn_s_setprio(1);
    u32 cw = __builtin_amdgcn_raw_buffer_load_b32(mkr(gc, D * 2), jobM ? at * 4 : OOB, 0, 0);
    u32 dpair = __builtin_amdgcn_raw_buffer_load_b32(mkr(gssmD, D * 2), j * 4, 0, 0);
    FENCE();
    if (PF0SLP) __builtin_amdgcn_s_sleep(PF0SLP);
    u32x2 wrw = __builtin_amdgcn_raw_buffer_load_b64(mkr(gwr, D * 2), (at & 127) * 8, 0, 0);
    const u16* pP0 = jobG ? gwd : gwmod;
    PINS(pP0);
    const rsrc_t rP0 = mkr(pP0, D * 6 * D * 2);
    const int pf_g = m * 57344 + at * 128, pf_m = (m * 16 + (at >> 3)) * 6144 + g * 1024 + (at & 7) * 128;
    const int pf_n = jobG ? 192 : (g < 6 ? 128 : 0);
    int o_pf0 = at < pf_n ? (jobG ? pf_g : pf_m) : OOB;
    u32 pf0;
    if (PFPOS == 0) pf0 = __builtin_amdgcn_raw_buffer_load_b32(rP0, o_pf0, 0, NT);
    FENCE();
    EPOCH();
    MAKE_TAG();
    int o_own = j * 8192 + at * 16;
    int o_m = (a * 128 + (lane >> 2)) * 6144 + mcol * 2 + (lane & 3) * 16;
    int o_w = L_W + a * 8192 + ((lane >> 4) * 8 + ((lane & 15) >> 2)) * 64 + (lane & 3) * 8;
    asm volatile("" : "+v"(o_own), "+v"(o_m), "+v"(o_w), "+v"(s4), "+v"(o_pf0));
    publish_kv(Int<PFPOS == 0 ? 4 : 3>{}, [&]() __attribute__((always_inline)) {
      if (PFPOS == 1) { pf0 = __builtin_amdgcn_raw_buffer_load_b32(rP0, o_pf0, 0, NT); FENCE(); }
    });
    asm volatile("" : "+v"(dpair), "+v"(cw), "+v"(wrw) :: "memory");
    u32x4 av[4];
    auto mod_request = [&]() __attribute__((always_inline)) {
      const rsrc_t rM = mkr(gwmod, D * 6 * D * 2);
#pragma unroll
      for (int i = 0; i < 8; ++i) dma16(rM, o_m, i * 16 * 6144, ldsb + L_W + a * 8192 + i * 1024);
    };
    auto mod_compute = [&]() __attribute__((always_inline)) {
      STAMP(21);
      u32x4 bw[8];
#pragma unroll
      for (int i = 0; i < 8; ++i) bw[i] = trfrag(lds + o_w + (i >> 1) * 2048 + (i & 1) * 32, 256);
      PIN8(bw);
      f32x4 d0 = {0, 0, 0, 0}, d1 = {0, 0, 0, 0};
#pragma unroll
      for (int ks = 0; ks < 4; ++ks) { d0 = mfma(av[ks], bw[2 * ks], d0); d1 = mfma(av[ks], bw[2 * ks + 1], d1); }
      if (lane < 16) { *(float*)(lds + L_MP + a * 128 + lane * 4) = d0[0]; *(float*)(lds + L_MP + a * 128 + 64 + lane * 4) = d1[0]; }
      const u32 nth = arrive(cnt, 2);
      {
        f32x4 pp[4];
#pragma unroll
        for (int i = 0; i < 4; ++i) pp[i] = *(const f32x4*)(lds + L_MP + i * 128 + (lane & 7) * 16);
        float sm[4];
#pragma unroll
        for (int e = 0; e < 4; ++e) sm[e] = add1(add1(pp[0][e], pp[1][e]), add1(pp[2][e], pp[3][e]));
        const u32 p0 = pk(sm[0], sm[1]), p1 = pk(sm[2], sm[3]);
        st128((u32x4){p0, p1, p0 ^ tagv.x, p1 ^ tagv.y}, rW, nth == 3 ? OFF_MOD + (lane >> 3) * 12288 + mcol * 4 + (lane & 7) * 16 : OOB);
        FENCE();
      }
      STAMP(3);
    };
    if (jobM) {
      if (MPOS == 0) { await<1>(cnt, 12, 4); mod_request(); }
      f32x2 cv = up2(cw);
      cv.x = mul1(cv.x, __builtin_amdgcn_rcpf(add1(1.f, __builtin_amdgcn_exp2f(mul1(cv.x, -1.44269504088896f)))));
      cv.y = mul1(cv.y, __builtin_amdgcn_rcpf(add1(1.f, __builtin_amdgcn_exp2f(mul1(cv.y, -1.44269504088896f)))));
      *(u32*)(lds + L_SC + at * 4) = pk2(cv);
#pragma unroll
      for (int ks = 0; ks < 4; ++ks) av[ks] = *(const u32x4*)(lds + L_SC + a * 256 + ks * 64 + (lane >> 4) * 16);
      PIN(av[0]); PIN(av[1]); PIN(av[2]); PIN(av[3]);
      if (MPOS == 2) { await<1>(cnt, 12, 4); mod_request(); }
      if (MPOS == 0 || MPOS == 2) { drain(); mod_compute(); }
    }
    int o_sm = OOB, o_q = row * 1024 + hl * 16;
    if (at < 16) o_sm = OFF_RK + (at >> 1) * 256 * RKS + j * RKS + (at & 1) * 16;
    if (jobF && at >= 16 && at < 32) o_sm = OFF_GU + f0 * 16 + (at - 16) * 16;
    int o_pf = jobG ? m * 57344 + 24576 + at * 128 : (a < 2 ? (m * 16 + (lane >> 2)) * (F * 2) + g * 512 + (lane & 3) * 128 : OOB);
    const int un = 128 * a + 4 * (lane & 15) + (lane >> 4);
    int o_u = L_U + (30 + (un >> 1)) * 16 + ((un & 1) ? 0 : 8);
    int o_mod = OFF_MOD + g * 12288 + (a >> 1) * 6144 + (at & 127) * 16;
    asm volatile("" : "+v"(o_sm), "+v"(o_q), "+v"(o_pf), "+v"(o_u), "+v"(o_mod));
    await<1>(cnt, 3, 4);
    STAMP(6);
    PIN(pf0);
    if (MPOS == 1 && jobM) { mod_request(); if (arrive(cnt, 12) != 3) await<1>(cnt, 12, 4); }
    __builtin_amdgcn_raw_buffer_store_b128(s4, rW, o_own, OFF_H2, SC1);
    __builtin_amdgcn_raw_buffer_store_b128(s4, rW, o_own, OFF_H2 + 4096, SC1);
    st128(s4, rW, o_sm);
    FENCE();
    u32x4 q0 = ld128(mkr(gq, S * D * 2), o_q, NT), q1 = ld128(mkr(gq, S * D * 2), o_q + 512, NT);
    FENCE();
    u32 pf1 = __builtin_amdgcn_raw_buffer_load_b32(mkr(jobG ? gwd : (a == 0 ? gwg : gwu), D * F * 2), o_pf, 0, NT);
    FENCE();
    {
      const f32x2 dv = up2(dpair);
#pragma unroll
      for (int t = 0; t < 2; ++t) {
        const u32x2 ua = *(const u32x2*)(lds + o_u + 512 * t), ub = *(const u32x2*)(lds + o_u + 512 * t + 4608);
        char* dp = lds + L_KD + (2 * a + t) * 1024 + lane * 16;
        *(f32x4*)(dp) = (f32x4){mul0(hi2f(ua.y), dv.x), mul0(lo2f(ua.y), dv.x), mul0(hi2f(ua.x), dv.x), mul0(lo2f(ua.x), dv.x)};
        *(f32x4*)(dp + 8192) = (f32x4){mul0(hi2f(ub.y), dv.y), mul0(lo2f(ub.y), dv.y), mul0(hi2f(ub.x), dv.y), mul0(lo2f(ub.x), dv.y)};
      }
    }
    if (MPOS == 1 && jobM) {
      asm volatile("s_waitcnt vmcnt(6)" ::: "memory");
      mod_compute();
      asm volatile("s_waitcnt vmcnt(4)" ::: "memory");
    } else {
      asm volatile("s_waitcnt vmcnt(3)" ::: "memory");
    }
    signal(cnt, 14);
    STAMP(5);
    asm volatile("" : "+v"(q0), "+v"(q1) :: "memory");
    STAMP(7);
    __builtin_amdgcn_s_setprio(0);
    f32x2 x[8], gah[8];
    {
      unpack16(q0, q1, x);
      float s = 0;
#pragma unroll
      for (int e = 0; e < 8; ++e) s = add0(s, add0(x[e].x, x[e].y));
      const float mean = half_sum(s) * (1.f / 512.f);
      float qq = 0;
#pragma unroll
      for (int e = 0; e < 8; ++e) { const float d0 = add0(x[e].x, -mean), d1 = add0(x[e].y, -mean); qq = fma0(d0, d0, qq); qq = fma0(d1, d1, qq); }
      const float var = rb(half_sum(qq) * (1.f / 512.f));
      float isd = __builtin_amdgcn_rcpf(rb(__builtin_amdgcn_sqrtf(rb(var + 1e-5f))));
      float mi = -rb(mean) * isd;
      PIN(isd); PIN(mi);
#pragma unroll
      for (int e = 0; e < 8; ++e) gah[e] = (f32x2){fma0(x[e].x, isd, mi), fma0(x[e].y, isd, mi)};
      PIN8(x); PIN8(gah);
    }
    STAMP(8);
    asm volatile("" : "+v"(pf1) :: "memory");
    if (F1POS == 1 && jobF) {
#pragma unroll
      for (int i = 0; i < 4; ++i) dma16(mkr(gwg, D * F * 2), (a * 128 + (lane >> 1)) * (F * 2) + f0 * 2 + (lane & 1) * 16, i * 32 * F * 2, ldsb + L_W + a * 4096 + i * 1024);
    }
    STAMP(4);
    if (MRD == 0) await<1>(cnt, 9, 4);
    if (MODSLP) __builtin_amdgcn_s_sleep(MODSLP);
    {
      u32x4 mu[3];
#pragma unroll
      for (int i = 0; i < 3; ++i) mu[i] = ld128(rW, o_mod + i * 2048, SC1);
      auto modbad = [&]() __attribute__((always_inline)) {
        u32 d = 0;
#pragma unroll
        for (int i = 0; i < 3; ++i) d |= (mu[i].x ^ mu[i].z ^ tagv.x) | (mu[i].y ^ mu[i].w ^ tagv.y);
        return d != 0;
      };
      bool nb = modbad();
      RETRY(__any(nb)) {
        FENCE();
        __builtin_amdgcn_s_sleep(3);
        if (nb) {
#pragma unroll
          for (int i = 0; i < 3; ++i) mu[i] = ld128(rW, o_mod + i * 2048, SC1);
        }
        asm volatile("" : "+v"(mu[0]), "+v"(mu[1]), "+v"(mu[2]));
        nb = modbad();
      }
      __builtin_amdgcn_s_setprio(1);
      STAMP(17);
      const f32x2 s0 = up2(mu[0].x), s1 = up2(mu[0].y), c0 = rb2(up2(mu[1].x) + 1.f), c1 = rb2(up2(mu[1].y) + 1.f), g0 = up2(mu[2].x), g1 = up2(mu[2].y);
      char* mo = lds + L_MOD + (at & 1) * 1024 + ((at & 127) >> 1) * 16;
      auto put = [&](int v, f32x2 lo, f32x2 hi) __attribute__((always_inline)) { *(f32x4*)(mo + v * 2048) = (f32x4){lo.x, lo.y, hi.x, hi.y}; };
      if (a < 2) {
        put(0, g0 * c0, g1 * c1);
        put(1, g0 * s0, g1 * s1);
      } else {
        const f32x2 w0 = up2(wrw.x), w1 = up2(wrw.y), r0 = c0 * w0, r1 = c1 * w1, sa_ = r0 + r1, sc_ = s0 * w0 + s1 * w1;
        put(2, r0, r1); put(3, c0, c1); put(4, s0, s1); put(5, g0, g1);
        const float pa = half_sum(sa_.x + sa_.y), pc2 = half_sum(sc_.x + sc_.y);
        if (hl == 0) *(f32x2*)(lds + L_MOD + 12288 + ((a & 1) * 2 + (lane >> 5)) * 8) = (f32x2){pa, pc2};
      }
      STAMP(9);
      if (arrive(cnt, 13) != 3) await<1>(cnt, 13, 4);
    }
    f32x2 ra[8], cb1[8], sb[8], gb[8];
    float suma, sumc;
    {
      const char* mp = lds + L_MOD + hl * 16;
      auto vec16 = [&](int v, f32x2* o) __attribute__((always_inline)) {
#pragma unroll
        for (int i = 0; i < 4; ++i) {
          const f32x4 t = *(const f32x4*)(mp + v * 2048 + (i >> 1) * 512 + (i & 1) * 1024);
          o[2 * i] = (f32x2){t[0], t[1]}; o[2 * i + 1] = (f32x2){t[2], t[3]};
        }
      };
      f32x2 a1[8], a0[8];
      vec16(0, a1); vec16(1, a0); vec16(2, ra); vec16(3, cb1); vec16(4, sb); vec16(5, gb);
      const f32x4 pa = *(const f32x4*)(lds + L_MOD + 12288), pc2 = *(const f32x4*)(lds + L_MOD + 12288 + 16);
#pragma unroll
      for (int e = 0; e < 8; ++e) gah[e] = gah[e] * a1[e] + a0[e];
      suma = (pa[0] + pa[2]) + (pc2[0] + pc2[2]);
      sumc = (pa[1] + pa[3]) + (pc2[1] + pc2[3]);
      PIN8(gah); PIN8(ra); PIN8(cb1); PIN8(sb); PIN8(gb); PIN(suma); PIN(sumc);
    }
    int o_key = hl < 8 ? OFF_RK + hl * 256 * RKS + j * RKS + (row & 7) * 4 : OOB;
    PIN(o_key);
    STAMP(10);
    bar();
    STAMP(11);
    {
      f32x2 z[8];
      const char* zr = lds + L_Z + (2 * a + (lane >> 5)) * 1040 + hl * 16;
      unpack16(*(const u32x4*)(zr), *(const u32x4*)(zr + 512), z);
      f32x2 s1 = {0, 0}, s2 = {0, 0}, s3 = {0, 0};
#pragma unroll
      for (int e = 0; e < 8; ++e) { x[e] += gah[e] * z[e]; s1 += x[e]; s2 += x[e] * x[e]; s3 += x[e] * ra[e]; }
      const float t1 = half_sum(s1.x + s1.y), t2 = half_sum(s2.x + s2.y), t3 = half_sum(s3.x + s3.y);
      const float mean = t1 * (1.f / 512.f);
      const f32x2 mvb = rb2((f32x2){mean, fmaxf(t2 * (1.f / 512.f) - mean * mean, 0.f)});
      const float mb = mvb.x, isd = __builtin_amdgcn_rcpf(rb(__builtin_amdgcn_sqrtf(rb(mvb.y + 1e-5f))));
      const float rr = (t3 - mb * suma) * isd + sumc;
      u32 kb = pk(0.f, rr);
      kb ^= ((int)kb < 0) ? 0xffff0000u : 0x80000000u;
      __builtin_amdgcn_raw_buffer_store_b32(kb | 0x8000u | (u32)(2047 - row), rW, o_key, 0, SC1);
      signal(cnt, 10);
      STAMP(12);
      u32 hw[8], ow[8];
#pragma unroll
      for (int e = 0; e < 8; ++e) {
        const f32x2 h2 = (x[e] - mb) * isd * cb1[e] + sb[e];
        hw[e] = nos(pk2(h2));
        ow[e] = pk2(x[e] + gb[e] * h2);
      }
      PIN8(hw); PIN8(ow);
      drain();
      bar();
      STAMP(13);
      st128((u32x4){hw[0], hw[1], hw[2], hw[3]}, rW, OFF_H2 + row * 1024 + hl * 16);
      st128((u32x4){hw[4], hw[5], hw[6], hw[7]}, rW, OFF_H2 + row * 1024 + 512 + hl * 16);
      st128((u32x4){ow[0], ow[1], ow[2], ow[3]}, rO, row * 1024 + hl * 16);
      st128((u32x4){ow[4], ow[5], ow[6], ow[7]}, rO, row * 1024 + 512 + hl * 16);
    }
    if (b == 0 && a == 0) __builtin_amdgcn_raw_buffer_store_b64(tagv, rW, lane == 0 ? OFF_EP : OOB, 0, SC1);
    if (jobF || jobG) { bar(); bar(); }
    drain();
    STAMP(14);
    if (arrive(cnt, 5) == 3) __builtin_amdgcn_raw_buffer_store_b64(tagv, rW, lane == 0 ? OFF_FL3 + j * 128 : OOB, 0, SC1);
    if (jobG && a < 2) {
      __builtin_amdgcn_s_setprio(0);
      bar();
      const int r = lane & 15, tok = at >> 4;
      const u32 key = *(const u32*)(lds + L_SEL + tok * 4);
      const int orow = 2047 - (int)(key & 0x7ffu);
#pragma unroll 1
      for (int spins = 0; spins < SPIN_CAP; ++spins) {
        FENCE();
        const u32x2 f = __builtin_amdgcn_raw_buffer_load_b64(rW, OFF_FL3 + (orow >> 3) * 128, 0, SC1);
        if (__all(f.x == tagv.x && f.y == tagv.y)) break;
        __builtin_amdgcn_s_sleep(4);
      }
      FENCE();
      float oold = lo2f(__builtin_amdgcn_raw_buffer_load_b16(rO, orow * 1024 + (gd0 + r) * 2, 0, SC1));
      u32 rbits = key >> 16;
      rbits = (rbits & 0x8000u) ? (rbits & 0x7fffu) : (rbits ^ 0xffffu);
      float gy = rb(__builtin_amdgcn_rcpf(1.f + __expf(-lo2f(rbits)))) * *(const float*)(lds + L_MOD + 10240 + ((r >> 2) & 1) * 1024 + ((gd0 + r) >> 3) * 16 + (r & 3) * 4);
      int oo = (r & 1) ? OOB : orow * 1024 + (gd0 + r) * 2, po = L_KC + ((tok >> 2) * 16 + r) * 16 + (tok & 3) * 4;
      PIN(gy); PIN(oold); PIN(oo); PIN(po);
      STAMP(15);
      bar();
      float y = 0;
#pragma unroll
      for (int i = 0; i < 8; ++i) y += *(const float*)(lds + po + i * 512);
      const float o = oold + gy * rb(y);
      const float on = dppf<DPP_X1>(o);
      __builtin_amdgcn_raw_buffer_store_b32(pk(o, on), rO, oo, 0, SC1);
      STAMP(16);
    }
    FLUSH();
  }
}

extern "C" void kernel_launch(void* const* d_in, const int* in_sizes, int n_in, void* d_out, int out_size, void* d_ws,
                              size_t ws_size, hipStream_t stream) {
  fused_kernel<<<256, 512, 0, stream>>>((char*)d_ws + (WSEND ? ((ws_size - WS_BYTES) & ~(size_t)4095) : 0), desc_hi(WS_BYTES), (const u16*)d_in[1], desc_hi(S * D * 2), (const u16*)d_in[2],
                                        desc_hi(S * D * 2), (const u16*)d_in[3], desc_hi(D * 8), (const u16*)d_in[4], (const u16*)d_in[11],
                                        (const u16*)d_in[12], (const u16*)d_in[0], (const u16*)d_in[5], (const u16*)d_in[7],
                                        (const u16*)d_in[8], (const u16*)d_in[9], (const u16*)d_in[10], (u16*)d_out);
}
```

```cpp
#define PROBE 0
#include <hip/hip_runtime.h>
#include <stdint.h>

typedef unsigned short u16;
typedef unsigned int u32;
typedef unsigned long long u64;
typedef short bf16x8 __attribute__((ext_vector_type(8)));
typedef short s16x4 __attribute__((ext_vector_type(4)));
typedef float f32x4 __attribute__((ext_vector_type(4)));
typedef float f32x2 __attribute__((ext_vector_type(2)));
typedef __bf16 bf16x2 __attribute__((ext_vector_type(2)));
typedef u32 u32x4 __attribute__((ext_vector_type(4)));
typedef u32 u32x2 __attribute__((ext_vector_type(2)));
typedef __amdgpu_buffer_rsrc_t rsrc_t;
#define LAS __attribute__((address_space(3)))

constexpr int S = 2048, D = 512, F = 1792;
constexpr u32 SENT = 0xFFFFFFFFu;
constexpr int SC1 = 16, NT = 2;
constexpr int SPIN_CAP = 1 << 14;
#define RKS 32
#define JOBPOS 0
#define FW0 4
#define CHKSLP 4
#define ZSLP 0
#define MPOS 0
#define E1SLP 0
#define MRD 1
#define PFPOS 0
#define F1POS 0
#define WSEND 0
#define KSLP 0
#define MODSLP 0
#define F2S 8
#define SSMPOS 0
#define F1S 8
#define PF0SLP 0
#define GDELAY 200

constexpr int OFF_E1 = 0;
constexpr int OFF_E2 = 2 << 20;
constexpr int OFF_H2 = 4 << 20;
constexpr int OFF_SM = 6 << 20;
constexpr int OFF_MOD = OFF_SM + 524288;
constexpr int OFF_RK = 7 << 20;
constexpr int OFF_GU = OFF_SM + 65536;
constexpr int OFF_CK = OFF_SM + 262144;
constexpr int OFF_CZ = (7 << 20) + 524288;
constexpr int OFF_EP = OFF_SM + 131072;
constexpr int OFF_FL3 = OFF_SM + 196608;
constexpr int OFF_ST = 8 << 20;
constexpr int WS_BYTES = 16 << 20;
constexpr int OOB = 0x7ffffff0;

constexpr int KC_STRIDE = 4160;
constexpr int L_KC = 0;
constexpr int L_U = 33280;
constexpr int L_Z = L_U + 9216;
constexpr int L_W = L_Z + 8320;
constexpr int L_KV = L_W + 57344;
constexpr int L_KD = L_KV;
constexpr int L_SC = L_KV + 18432;
constexpr int L_MOD = L_SC + 1024;
constexpr int L_MP = L_MOD + 12288 + 32;
constexpr int L_GM = L_MP + 512;
constexpr int L_CNT = L_GM + 64;
constexpr int L_RANK = L_CNT + 64;
constexpr int L_SEL = L_RANK + 64;
constexpr int L_LIST = L_SEL + 32;
constexpr int L_END = L_LIST + 4096;

#define STAMP(i) do { if ((i) == PROBE) tprobe = __builtin_amdgcn_s_memrealtime(); } while (0)
#define FLUSH() do { } while (0)

template <int N> struct Int { static constexpr int value = N; };
#define PIN(x) asm volatile("" : "+v"(x))
#define PINS(x) asm volatile("" : "+s"(x))
#define PIN8(a) asm volatile("" : "+v"((a)[0]), "+v"((a)[1]), "+v"((a)[2]), "+v"((a)[3]), "+v"((a)[4]), "+v"((a)[5]), "+v"((a)[6]), "+v"((a)[7]))
#define FENCE() asm volatile("" ::: "memory")
#define RETRY(bad) if (__builtin_expect(bad, 0)) _Pragma("unroll 1") for (int spins = 0; spins < SPIN_CAP && (spins == 0 || (bad)); ++spins)
#define SCHED() __builtin_amdgcn_sched_barrier(0)
__device__ __forceinline__ float lo2f(u32 w) { return __uint_as_float(w << 16); }
__device__ __forceinline__ float hi2f(u32 w) { return __uint_as_float(w & 0xffff0000u); }
__device__ __forceinline__ f32x2 up2(u32 w) { return (f32x2){lo2f(w), hi2f(w)}; }
__device__ __forceinline__ u32 pk2(f32x2 v) { return __builtin_bit_cast(u32, __builtin_convertvector(v, bf16x2)); }
__device__ __forceinline__ u32 pk(float lo, float hi) { return pk2((f32x2){lo, hi}); }
__device__ __forceinline__ u32 nos(u32 x) { return min(x, SENT - 1u); }
__device__ __forceinline__ float rb(float x) { return (float)(__bf16)x; }
__device__ __forceinline__ f32x2 rb2(f32x2 v) { return up2(pk2(v)); }
__device__ __forceinline__ float mul1(float a, float b) { float r; asm("s_nop 0\n\tv_mul_f32 %0, %1, %2" : "=v"(r) : "v"(a), "v"(b)); return r; }
__device__ __forceinline__ float add1(float a, float b) { float r; asm("s_nop 0\n\tv_add_f32 %0, %1, %2" : "=v"(r) : "v"(a), "v"(b)); return r; }
__device__ __forceinline__ float mul0(float a, float b) { float r; asm("v_mul_f32 %0, %1, %2" : "=v"(r) : "v"(a), "v"(b)); return r; }
__device__ __forceinline__ float add0(float a, float b) { float r; asm("v_add_f32 %0, %1, %2" : "=v"(r) : "v"(a), "v"(b)); return r; }
__device__ __forceinline__ float fma0(float a, float b, float c) { float r; asm("v_fma_f32 %0, %1, %2, %3" : "=v"(r) : "v"(a), "v"(b), "v"(c)); return r; }
__device__ __forceinline__ u32 fold2(u32 a, u32 b) { return a + __builtin_amdgcn_alignbit(b, b, 21); }
__device__ __forceinline__ u32 umax4(u32x4 x) { return max(max(x.x, x.y), max(x.z, x.w)); }
__device__ __forceinline__ void bar() { asm volatile("s_waitcnt lgkmcnt(0)\n\ts_barrier" ::: "memory"); }
__device__ __forceinline__ void drain() { asm volatile("s_waitcnt vmcnt(0)" ::: "memory"); }
__device__ __forceinline__ rsrc_t mkr(const void* p, int bytes) { return __builtin_amdgcn_make_buffer_rsrc((void*)p, 0, bytes, 0x00020000); }
__device__ __forceinline__ u32x4 ld128(rsrc_t r, int off, int aux) {
  return aux == SC1 ? __builtin_amdgcn_raw_buffer_load_b128(r, off, 0, SC1)
                    : (aux == NT ? __builtin_amdgcn_raw_buffer_load_b128(r, off, 0, NT) : __builtin_amdgcn_raw_buffer_load_b128(r, off, 0, 0));
}
__device__ __forceinline__ void st128(u32x4 v, rsrc_t r, int off) { __builtin_amdgcn_raw_buffer_store_b128(v, r, off, 0, SC1); }
__device__ __forceinline__ f32x4 mfma(u32x4 a, u32x4 b, f32x4 c) {
  return __builtin_amdgcn_mfma_f32_16x16x32_bf16(__builtin_bit_cast(bf16x8, a), __builtin_bit_cast(bf16x8, b), c, 0, 0, 0);
}
__device__ __forceinline__ void dma16(rsrc_t r, int voff, int soff, u32 ldsaddr) {
  asm volatile("s_mov_b32 m0, %2\n\ts_nop 0\n\tbuffer_load_dwordx4 %0, %1, %3 offen nt lds" :: "v"(voff), "s"(r), "s"(ldsaddr), "s"(soff) : "memory");
}
__device__ __forceinline__ u32x2 trread(const char* p) {
  return __builtin_bit_cast(u32x2, __builtin_amdgcn_ds_read_tr16_b64_v4i16((LAS s16x4*)(LAS char*)p));
}
__device__ __forceinline__ float dot2(u32 a, u32 b, float c) {
  return __builtin_amdgcn_fdot2_f32_bf16(__builtin_bit_cast(bf16x2, a), __builtin_bit_cast(bf16x2, b), c, false);
}
__device__ __forceinline__ void dma16_sc1(rsrc_t r, int voff, int soff, u32 ldsaddr) {
  asm volatile("s_mov_b32 m0, %2\n\ts_nop 0\n\tbuffer_load_dwordx4 %0, %1, %3 offen sc1 lds" :: "v"(voff), "s"(r), "s"(ldsaddr), "s"(soff) : "memory");
}
__device__ __forceinline__ u32x4 trfrag(const char* p, int q4) {
  const u32x2 a = __builtin_bit_cast(u32x2, __builtin_amdgcn_ds_read_tr16_b64_v4i16((LAS s16x4*)(LAS char*)p));
  const u32x2 c = __builtin_bit_cast(u32x2, __builtin_amdgcn_ds_read_tr16_b64_v4i16((LAS s16x4*)(LAS char*)(p + q4)));
  return (u32x4){a.x, a.y, c.x, c.y};
}
template <int CTRL> __device__ __forceinline__ u32 dppu(u32 x) { return (u32)__builtin_amdgcn_mov_dpp((int)x, CTRL, 0xf, 0xf, true); }
template <int CTRL> __device__ __forceinline__ float dppf(float x) { return __uint_as_float(dppu<CTRL>(__float_as_uint(x))); }
constexpr int DPP_X1 = 0xB1, DPP_X2 = 0x4E, DPP_HM = 0x141, DPP_M = 0x140;
__device__ __forceinline__ float half_sum(float v) {
  v += dppf<DPP_X1>(v); v += dppf<DPP_X2>(v); v += dppf<DPP_HM>(v); v += dppf<DPP_M>(v);
  auto s = __builtin_amdgcn_permlane16_swap(__float_as_uint(v), __float_as_uint(v), false, false);
  return __uint_as_float(s[0]) + __uint_as_float(s[1]);
}
__device__ __forceinline__ void unpack16(u32x4 a, u32x4 c, f32x2* o) {
#pragma unroll
  for (int e = 0; e < 4; ++e) { o[e] = up2(a[e]); o[4 + e] = up2(c[e]); }
}
__device__ __forceinline__ u32 rank16(const char* lds, int lane, u32& mine) {
  mine = *(const u32*)(lds + (lane & 15) * 4);
  const u32x4 o = *(const u32x4*)(lds + (lane >> 4) * 16);
  u32 c = (o.x > mine) + (o.y > mine) + (o.z > mine) + (o.w > mine);
  auto s = __builtin_amdgcn_permlane16_swap(c, c, false, false);
  c = s[0] + s[1];
  auto t = __builtin_amdgcn_permlane32_swap(c, c, false, false);
  return t[0] + t[1];
}
__device__ __forceinline__ u32 arrive(u32 cnt, int idx) {
  u32 old, tmp;
  asm volatile("s_waitcnt lgkmcnt(0)\n\ts_mov_b64 exec, 1\n\tv_mov_b32 %1, 1\n\tds_add_rtn_u32 %1, %2, %1 offset:%3\n\ts_mov_b64 exec, -1\n\t"
               "s_waitcnt lgkmcnt(0)\n\tv_readfirstlane_b32 %0, %1" : "=s"(old), "=&v"(tmp) : "v"(cnt), "i"(idx * 4) : "memory");
  return old;
}
__device__ __forceinline__ void signal(u32 cnt, int idx) {
  u32 tmp;
  asm volatile("s_waitcnt lgkmcnt(0)\n\ts_mov_b64 exec, 1\n\tv_mov_b32 %0, 1\n\tds_add_u32 %1, %0 offset:%2\n\ts_mov_b64 exec, -1"
               : "=&v"(tmp) : "v"(cnt), "i"(idx * 4) : "memory");
}
__device__ __forceinline__ u32 peek(u32 cnt, int idx) {
  u32 v, tmp;
  asm volatile("ds_read_b32 %1, %2 offset:%3\n\ts_waitcnt lgkmcnt(0)\n\tv_readfirstlane_b32 %0, %1" : "=s"(v), "=&v"(tmp) : "v"(cnt), "i"(idx * 4) : "memory");
  return v;
}
template <int SLEEP> __device__ __forceinline__ void await(u32 cnt, int idx, u32 n) {
#pragma unroll 1
  for (int spins = 0; peek(cnt, idx) < n && spins < (SPIN_CAP << 6); ++spins) if (SLEEP) __builtin_amdgcn_s_sleep(SLEEP);
}

typedef u32 u32x16 __attribute__((ext_vector_type(16)));
typedef u64 u64x8 __attribute__((ext_vector_type(8)));
typedef u64 u64x2 __attribute__((ext_vector_type(2)));
constexpr u64 desc_hi(u32 bytes) { return (u64)bytes | ((u64)0x00020000u << 32); }
#define SUB4(v, i) __builtin_shufflevector(v, v, 4 * (i), 4 * (i) + 1, 4 * (i) + 2, 4 * (i) + 3)

__global__ __launch_bounds__(512, 2) void fused_kernel(
    char* a0, u64 a1, const u16* a2, u64 a3, const u16* a4, u64 a5, const u16* a6, u64 a7, const u16* a8, const u16* a9,
    const u16* a10, const u16* a11, const u16* a12, const u16* a13, const u16* a14, const u16* a15, const u16* a16, u16* a17) {
  __shared__ __attribute__((aligned(16))) char lds[L_END];
  u32x16 ka0; u64x8 ka1; u64x2 ka2;
  asm volatile("s_load_dwordx16 %0, %3, 0x0\n\ts_load_dwordx16 %1, %3, 0x40\n\ts_load_dwordx4 %2, %3, 0x80"
               : "=&s"(ka0), "=&s"(ka1), "=&s"(ka2) : "s"(__builtin_amdgcn_kernarg_segment_ptr()));
  SCHED();
  const u64 tstart = __builtin_amdgcn_s_memrealtime();
  u64 tprobe = tstart;
  STAMP(0);
  const int tid = threadIdx.x, lane = tid & 63;
  const int w = __builtin_amdgcn_readfirstlane(tid >> 6);
  const int b = blockIdx.x, g = b & 7, m = b >> 3, j = 32 * g + m, cg = (g + 1) & 7;
  u32x4 s4 = {SENT, SENT, SENT, SENT};

  const u32 ldsb = (u32)(size_t)(LAS char*)lds;
  int kvo = ((tid >> 3) << 10) | ((((tid & 7) ^ ((tid >> 3) & 2) ^ ((tid >> 4) & 4)) ^ 4) << 4);
  u32 cls = OFF_E1 + cg * 262144 + m * 256, kvs = m * 65536 + cg * 128, kml = ldsb + L_KV + 1024 + w * 1024, vml = kml + 8192;
  int kho = (lane >= 40 && m > 0) ? ((kvo & 0x1fff) ^ 64) : OOB;
  int sko = lane < 32 ? lane * 16 : OOB;
  asm volatile("" : "+v"(kvo), "+v"(kho), "+v"(sko));
  asm volatile("" : "+s"(kvs), "+s"(kml), "+s"(vml));
  SCHED();
  asm volatile("s_waitcnt lgkmcnt(0)" : "+s"(ka0), "+s"(ka1), "+s"(ka2));
  asm volatile("s_mov_b32 m0, %3\n\ts_nop 0\n\tbuffer_load_dwordx4 %0, %1, %2 offen nt lds\n\t"
               "s_mov_b32 m0, %5\n\ts_nop 0\n\tbuffer_load_dwordx4 %0, %4, %2 offen nt lds"
               :: "v"(kvo), "s"(SUB4(ka0, 1)), "s"(kvs), "s"(kml), "s"(SUB4(ka0, 2)), "s"(vml) : "memory");
  if (w == 4)
    asm volatile("s_mov_b32 m0, %3\n\ts_nop 0\n\tbuffer_load_dwordx4 %0, %1, %2 offen nt lds"
                 :: "v"(kho), "s"(SUB4(ka0, 1)), "s"(m > 0 ? kvs - 8192 : 0u), "s"(ldsb + L_KV) : "memory");
  if (w == 5)
    asm volatile("s_mov_b32 m0, %3\n\ts_nop 0\n\tbuffer_load_dwordx4 %0, %1, %2 offen lds"
                 :: "v"(sko), "s"(SUB4(ka0, 3)), "s"(cg * 512), "s"(ldsb + L_KV + 17408) : "memory");
  SCHED();
  char* ws = (char*)(((u64)ka0[1] << 32) | ka0[0]);
  const u16 *gssmk = (const u16*)ka1[0], *gc = (const u16*)ka1[1], *gwmod = (const u16*)ka1[2], *gq = (const u16*)ka1[3],
            *gssmD = (const u16*)ka1[4], *gwr = (const u16*)ka1[5], *gwg = (const u16*)ka1[6], *gwu = (const u16*)ka1[7], *gwd = (const u16*)ka2[0];
  u16* gout = (u16*)ka2[1];
  const rsrc_t rW = mkr(ws, WS_BYTES);
  const bool jobG = g == 7, jobF = g < 7 && m >= 16, jobM = g < 6 && m < 16;
  const int mcol = g * 512 + m * 32;
  int e1_out = cls + ((w & 3) * 8 + ((lane & 15) >> 1)) * 8192 + (w >> 2) * 128 + (lane & 1) * 64 + (lane >> 4) * 16;
  STAMP(16);
  if (w >= 6) {
    const int at = tid - 384;
    int zo = L_CNT + (at & 3) * 16;
    if (at < 60) zo = L_U + (at >= 30 ? 4608 - 480 : 0) + at * 16;
    if (at >= 64 && at < 80) zo = L_KC + ((at >> 1) & 7) * KC_STRIDE + (at & 1) * 16;
    if (at >= 80 && at < 84) zo = L_LIST + (at & 3) * 16;
    *(u32x4*)(lds + zo) = (u32x4){0, 0, 0, 0};
  }
  const int f0 = g * 256 + (m - 16) * 16;
  const int gd0 = m * 16;
  u32 cnt = ldsb + L_CNT;
  PIN(cnt);
  u64 ep;
#define EPOCH() asm volatile("s_load_dwordx2 %0, %1, %2\n\ts_waitcnt lgkmcnt(0)" : "=&s"(ep) : "s"(ws), "s"(OFF_EP) : "memory"); STAMP(18)
  u32x2 tagv;
#define MAKE_TAG() do { const u64 tag = ep + 1ull; tagv = (u32x2){(u32)tag, (u32)(tag >> 32)}; } while (0)
  auto publish_kv = [&](auto younger, auto hook) __attribute__((always_inline)) {
    const int blk = 4 * (w >> 2) + (lane >> 4) + 1;
    int ko = (blk * 8 + ((lane & 15) >> 2)) * 128 + (((w & 3) ^ ((lane >> 3) & 1) ^ ((blk & 1) << 1)) << 5) + (lane & 3) * 8;
    int cko = (lane & 17) == 0 ? OFF_CK + ((cg * 4 + (w & 3)) * 64 + m * 2 + (w >> 2)) * 64 + (lane & 14) * 4 + (lane >> 5) * 4 : OOB;
    u32 tw = lane < 32 ? tagv.x : tagv.y;
    PIN(ko); PIN(e1_out); PIN(cko); PIN(tw);
    int sko2 = L_KV + 17408 + ((w & 3) * 16 + (lane & 15)) * 8, ko2 = L_KV - 512 + (ko ^ 64);
    PIN(sko2); PIN(ko2);
    asm volatile("s_waitcnt vmcnt(%0)\n\ts_barrier" :: "i"(younger.value) : "memory");
    STAMP(20);
    hook();
    const char* kp = lds + L_KV + ko;
    const u32x2 sk = *(const u32x2*)(lds + sko2);
    const u32x2 p0 = trread(lds + ko2), p1 = trread(kp), p2 = trread(kp + 512), v0 = trread(kp + 8192), v1 = trread(kp + 8704);
    const u32 c10 = __builtin_amdgcn_alignbyte(sk.x, sk.x, 2), c32 = __builtin_amdgcn_alignbyte(sk.y, sk.y, 2);
    const u32 c0z = sk.x & 0xffffu, cz3 = sk.y & 0xffff0000u, c21 = (sk.y & 0xffffu) | (sk.x & 0xffff0000u);
    const u32 P[6] = {p0.x, p0.y, p1.x, p1.y, p2.x, p2.y}, V[4] = {v0.x, v0.y, v1.x, v1.y};
    u32 o[4];
#pragma unroll
    for (int i = 0; i < 4; ++i) {
      const float ev = dot2(P[i + 2], c0z, dot2(P[i + 1], c21, dot2(P[i], cz3, 0.f)));
      const float od = dot2(P[i + 2], c10, dot2(P[i + 1], c32, 0.f));
      o[i] = pk(od * hi2f(V[i]), ev * lo2f(V[i]));
    }
    st128((u32x4){o[3], o[2], o[1], o[0]}, rW, e1_out);
    FENCE();
    {
      auto t = __builtin_amdgcn_permlane32_swap(fold2(o[3], o[2]), fold2(o[1], o[0]), false, false);
      u32 hs = t[0] + t[1];
      auto u = __builtin_amdgcn_permlane16_swap(hs, hs, false, false);
      hs = u[0] + u[1];
      hs += dppu<DPP_X1>(hs);
      __builtin_amdgcn_raw_buffer_store_b32(hs + tw, rW, cko, 0, SC1);
    }
    FENCE();
    STAMP(2);
  };
  const rsrc_t rO = mkr(gout, S * D * 2);

  if (w < 4) {
    const int ch = w >> 1, half = w & 1, r = lane & 15, h = lane >> 4;
    u32x2 kx;
    auto taps = [&]() __attribute__((always_inline)) {
      const rsrc_t rS = mkr(gssmk, S * D * 2);
      const u32 dst = ldsb + L_KC + ch * (4 * KC_STRIDE) + 32 + half * 2048;
      dma16(rS, lane * 16, j * 8192 + w * 2048, dst);
      dma16(rS, lane * 16, j * 8192 + w * 2048 + 1024, dst + 1024);
      kx = __builtin_amdgcn_raw_buffer_load_b64(rS, j * 8192 + w * 2048 + 2048, 0, NT);
      FENCE();
    };
    if (SSMPOS == 0) taps();
    EPOCH();
    MAKE_TAG();
    int cki = OFF_CK + ((g * 4 + (m >> 3)) * 64 + w * 8 + (lane >> 3)) * 64 + (m & 7) * 8, e1i = OFF_E1 + j * 8192 + tid * 16;
    int o_z = OFF_E2 + j * 8192 + (128 * w + 4 * r + h) * 16;
    int o_cz = OFF_CZ + j * 2048 + w * 512 + r * 16 + h * 4;
    u32 tw2 = (h & 1) ? tagv.y : tagv.x;
    PIN(cki); PIN(e1i); PIN(o_z); PIN(o_cz); PIN(tw2);
    publish_kv(Int<SSMPOS == 0 ? 3 : 0>{}, []() {});
    if (MPOS == 0 && jobM) signal(cnt, 12);
    if (SSMPOS == 1) taps();
    asm volatile("" : "+v"(kx) :: "memory");
    STAMP(21);
    {
      char* kc = lds + L_KC + ch * (4 * KC_STRIDE) + (half * 128 + lane + 2) * 16;
      u32x4 kd[2];
      u32x2 kn[2];
#pragma unroll
      for (int i = 0; i < 2; ++i) { kd[i] = *(const u32x4*)(kc + i * 1024); kn[i] = *(const u32x2*)(kc + i * 1024 + 16); }
      if (lane == 63) kn[1] = kx;
#pragma unroll
      for (int i = 0; i < 2; ++i) {
        const u32 a01 = __builtin_amdgcn_alignbyte(kd[i].y, kd[i].x, 2), a12 = __builtin_amdgcn_alignbyte(kd[i].z, kd[i].y, 2);
        const u32 a23 = __builtin_amdgcn_alignbyte(kd[i].w, kd[i].z, 2), a34 = __builtin_amdgcn_alignbyte(kn[i].x, kd[i].w, 2);
        const u32 a45 = __builtin_amdgcn_alignbyte(kn[i].y, kn[i].x, 2);
        *(u32x4*)(kc + i * 1024 + KC_STRIDE) = (u32x4){a01, a12, a23, a34};
        *(u32x4*)(kc + i * 1024 + 2 * KC_STRIDE) = (u32x4){kd[i].y, kd[i].z, kd[i].w, kn[i].x};
        *(u32x4*)(kc + i * 1024 + 3 * KC_STRIDE) = (u32x4){a12, a23, a34, a45};
        if (i == 0 && half == 0 && lane == 0) {
          *(u32*)(kc - 4 + KC_STRIDE) = kd[0].x << 16;
          *(u32*)(kc - 4 + 2 * KC_STRIDE) = kd[0].x;
          *(u32x2*)(kc - 8 + 3 * KC_STRIDE) = (u32x2){kd[0].x << 16, a01};
        }
      }
    }
    STAMP(22);
    if (ch == 0) { if (arrive(cnt, 6) != 1) await<0>(cnt, 6, 2); } else { if (arrive(cnt, 7) != 1) await<0>(cnt, 7, 2); }
    STAMP(3);
    if (E1SLP < 0) drain(); else if (E1SLP) __builtin_amdgcn_s_sleep(E1SLP);
    STAMP(4);
    u32x4 x0 = ld128(rW, e1i, SC1), x1 = ld128(rW, e1i + 4096, SC1);
    u32x2 c0 = __builtin_amdgcn_raw_buffer_load_b64(rW, cki, 0, SC1), c1 = __builtin_amdgcn_raw_buffer_load_b64(rW, cki + 2048, 0, SC1);
    FENCE();
    if (MPOS == 2 && jobM) signal(cnt, 12);
    u32x2 Al[32], Ah[32];
    {
      const u32 ka = ldsb + L_KC + ch * (4 * KC_STRIDE) + ((1 + r) & 3) * KC_STRIDE + 2 * (128 * half + 8 * h + ((1 + r) & ~3));
#define RD4(i) asm volatile("ds_read_b64 %0, %8 offset:%9\n\tds_read_b64 %1, %8 offset:%9+8\n\tds_read_b64 %2, %8 offset:%9+64\n\t" \
      "ds_read_b64 %3, %8 offset:%9+72\n\tds_read_b64 %4, %8 offset:%9+128\n\tds_read_b64 %5, %8 offset:%9+136\n\t" \
      "ds_read_b64 %6, %8 offset:%9+192\n\tds_read_b64 %7, %8 offset:%9+200" \
      : "=&v"(Al[i]), "=&v"(Ah[i]), "=&v"(Al[8 + i]), "=&v"(Ah[8 + i]), "=&v"(Al[16 + i]), "=&v"(Ah[16 + i]), "=&v"(Al[24 + i]), "=&v"(Ah[24 + i]) \
      : "v"(ka), "i"(512 * i) : "memory")
      RD4(0); RD4(1); RD4(2); RD4(3); RD4(4); RD4(5); RD4(6); RD4(7);
      asm volatile("s_waitcnt lgkmcnt(0)" ::: "memory");
      PIN8(Al); PIN8(Al + 8); PIN8(Al + 16); PIN8(Al + 24); PIN8(Ah); PIN8(Ah + 8); PIN8(Ah + 16); PIN8(Ah + 24);
    }
    STAMP(5);
    {
      auto bad = [&]() __attribute__((always_inline)) {
        u32 h[4] = {fold2(x0.x, x0.y), fold2(x0.z, x0.w), fold2(x1.x, x1.y), fold2(x1.z, x1.w)};
#pragma unroll
        for (int i = 0; i < 4; ++i) h[i] += dppu<DPP_X1>(h[i]);
#pragma unroll
        for (int i = 0; i < 4; ++i) h[i] += dppu<DPP_X2>(h[i]);
#pragma unroll
        for (int i = 0; i < 4; ++i) h[i] += dppu<DPP_HM>(h[i]);
        return (((h[0] + tagv.x) ^ c0.x) | ((h[1] + tagv.y) ^ c0.y) | ((h[2] + tagv.x) ^ c1.x) | ((h[3] + tagv.y) ^ c1.y)) != 0;
      };
      bool nb = bad();
      RETRY(__any(nb)) {
        FENCE();
        if (nb) {
          x0 = ld128(rW, e1i, SC1); x1 = ld128(rW, e1i + 4096, SC1);
          c0 = __builtin_amdgcn_raw_buffer_load_b64(rW, cki, 0, SC1); c1 = __builtin_amdgcn_raw_buffer_load_b64(rW, cki + 2048, 0, SC1);
        }
        asm volatile("" : "+v"(x0), "+v"(x1), "+v"(c0), "+v"(c1));
        nb = bad();
      }
      STAMP(1);
      char* u0 = lds + L_U + ((tid >> 2) & 1) * 4608 + (30 + (tid >> 3) * 4 + (tid & 3)) * 16;
      *(u32x4*)(u0) = x0;
      *(u32x4*)(u0 + 2048) = x1;
    }
    if (arrive(cnt, 3) != 3) await<0>(cnt, 3, 4);
    STAMP(6);
    {
      f32x4 acc[8];
#pragma unroll
      for (int t = 0; t < 8; ++t) acc[t] = (f32x4){0, 0, 0, 0};
      const char* ub = lds + L_U + ch * 4608 + (30 + 2 * r + 1 - 16 * half - h) * 16;
      char* rp = lds + L_KC + w * 8192 + lane * 16;
      u32x4 B0[8], B1[8];
#pragma unroll
      for (int t = 0; t < 8; ++t) B0[t] = *(const u32x4*)(ub + 512 * t);
#pragma unroll
      for (int t = 0; t < 8; ++t) B1[t] = *(const u32x4*)(ub + 512 * t - 64);
      SCHED();
#define AF(ww, p) (u32x4){Al[8 * (ww) + (p)].x, Al[8 * (ww) + (p)].y, Ah[8 * (ww) + (p)].x, Ah[8 * (ww) + (p)].y}
#pragma unroll
      for (int bp = 0; bp < 8; ++bp) {
#pragma unroll
        for (int t = 7; t >= bp; --t) acc[t] = mfma(AF(0, t - bp), B0[bp], acc[t]);
        SCHED();
      }
#pragma unroll
      for (int bp = 0; bp < 8; ++bp) {
        B0[bp] = *(const u32x4*)(ub + 512 * bp - 128);
#pragma unroll
        for (int t = 7; t >= bp; --t) acc[t] = mfma(AF(1, t - bp), B1[bp], acc[t]);
        SCHED();
      }
#pragma unroll
      for (int bp = 0; bp < 8; ++bp) {
        B1[bp] = *(const u32x4*)(ub + 512 * bp - 192);
#pragma unroll
        for (int t = 7; t >= bp; --t) acc[t] = mfma(AF(2, t - bp), B0[bp], acc[t]);
        SCHED();
      }
#pragma unroll
      for (int t = 0; t < 8; ++t) {
#pragma unroll
        for (int bp = 0; bp <= t; ++bp) {
          acc[t] = mfma(AF(3, t - bp), B1[bp], acc[t]);
          if (t >= 2 && bp == 1) { SCHED(); *(f32x4*)(rp + (t - 1) * 1024) = acc[t - 1]; if (t == 2) *(f32x4*)(rp) = acc[0]; SCHED(); }
        }
        SCHED();
      }
      *(f32x4*)(rp + 7 * 1024) = acc[7];
    }
    STAMP(7);
    if (arrive(cnt, 14) != 7) await<0>(cnt, 14, 8);
    u32x4 zt[2];
#pragma unroll
    for (int t = 0; t < 2; ++t) {
      const char* rp = lds + L_KC + (2 * w + t) * 1024 + lane * 16;
      const char* dp = lds + L_KD + (2 * w + t) * 1024 + lane * 16;
      const f32x4 ya = *(const f32x4*)(rp) + *(const f32x4*)(rp + 8192) + *(const f32x4*)(dp);
      const f32x4 yb = *(const f32x4*)(rp + 16384) + *(const f32x4*)(rp + 24576) + *(const f32x4*)(dp + 8192);
      zt[t] = (u32x4){pk(ya[0], yb[0]), pk(ya[1], yb[1]), pk(ya[2], yb[2]), pk(ya[3], yb[3])};
      st128(zt[t], rW, o_z + 1024 * t);
    }
    FENCE();
#pragma unroll
    for (int t = 0; t < 2; ++t) {
      auto u = __builtin_amdgcn_permlane16_swap(fold2(zt[t].x, zt[t].y), fold2(zt[t].z, zt[t].w), false, false);
      __builtin_amdgcn_raw_buffer_store_b32(u[0] + u[1] + tw2, rW, o_cz + 256 * t, 0, SC1);
    }
    signal(cnt, 9);
    STAMP(15);
    u32x4 Wf[14];
    const int wvo = jobG ? (w * 448 + (lane >> 1)) * 1024 + gd0 * 2 + (lane & 1) * 16 : (w * 128 + (lane >> 1)) * (F * 2) + f0 * 2 + (lane & 1) * 16;
    if (F1POS == 0 && jobF) {
#pragma unroll
      for (int i = 0; i < 4; ++i) dma16(mkr(gwg, D * F * 2), wvo, i * 32 * F * 2, ldsb + L_W + w * 4096 + i * 1024);
      asm volatile("s_waitcnt vmcnt(4)" ::: "memory");
    } else if (jobG) {
#pragma unroll
      for (int i = 0; i < 4; ++i) dma16(mkr(gwd, F * D * 2), wvo, i * 32 * 1024, ldsb + L_W + w * 14336 + i * 1024);
      asm volatile("s_waitcnt vmcnt(4)" ::: "memory");
    } else {
      drain();
    }
    STAMP(8);
    {
      const int zo = OFF_E2 + (tid >> 1) * 8192 + j * 32 + (tid & 1) * 16, zc = OFF_CZ + (tid >> 1) * 2048 + j * 8 + (tid & 1) * 4;
      u32x4 z0 = ld128(rW, zo, SC1), z1 = ld128(rW, zo + 128 * 8192, SC1);
      u32 k0 = __builtin_amdgcn_raw_buffer_load_b32(rW, zc, 0, SC1), k1 = __builtin_amdgcn_raw_buffer_load_b32(rW, zc + 128 * 2048, 0, SC1);
      const u32 tw3 = (lane & 1) ? tagv.y : tagv.x;
      auto bad = [&]() __attribute__((always_inline)) {
        const u32 a0 = fold2(z0.x, z0.y), a1 = fold2(z0.z, z0.w), b0 = fold2(z1.x, z1.y), b1 = fold2(z1.z, z1.w);
        const u32 sa0 = a0 + dppu<DPP_X1>(a0), sa1 = a1 + dppu<DPP_X1>(a1), sb0 = b0 + dppu<DPP_X1>(b0), sb1 = b1 + dppu<DPP_X1>(b1);
        u32 d = ((((lane & 1) ? sa1 : sa0) + tw3) ^ k0) | ((((lane & 1) ? sb1 : sb0) + tw3) ^ k1);
        return d;
      };
      u32 nb = bad();
      STAMP(17);
      RETRY(__any(nb != 0)) {
        FENCE();
        nb |= dppu<DPP_X1>(nb);
        if (nb) {
          z0 = ld128(rW, zo, SC1); z1 = ld128(rW, zo + 128 * 8192, SC1);
          k0 = __builtin_amdgcn_raw_buffer_load_b32(rW, zc, 0, SC1); k1 = __builtin_amdgcn_raw_buffer_load_b32(rW, zc + 128 * 2048, 0, SC1);
        }
        asm volatile("" : "+v"(z0), "+v"(z1), "+v"(k0), "+v"(k1));
        nb = bad();
      }
      STAMP(9);
      char* zp = lds + L_Z + (tid & 1) * 4160 + (tid >> 1) * 4;
#pragma unroll
      for (int e = 0; e < 4; ++e) { *(u32*)(zp + e * 1040) = z0[e]; *(u32*)(zp + e * 1040 + 512) = z1[e]; }
    }
    bar();
    if (!(jobF || jobG)) { FLUSH(); return; }
    await<2>(cnt, 10, 4);
    if (jobG) {
#pragma unroll
      for (int i = 4; i < 8; ++i) dma16(mkr(gwd, F * D * 2), wvo, i * 32 * 1024, ldsb + L_W + w * 14336 + i * 1024);
    } else {
#pragma unroll
      for (int i = 0; i < 4; ++i) dma16(mkr(gwu, D * F * 2), wvo, i * 32 * F * 2, ldsb + L_W + 16384 + w * 4096 + i * 1024);
    }
    bar();
    if (KSLP) __builtin_amdgcn_s_sleep(KSLP);
    STAMP(11);
    const u64 tb6 = __builtin_amdgcn_s_memrealtime();
    const int ko = OFF_RK + g * 256 * RKS + (tid >> 1) * RKS + (tid & 1) * 16;
    u32x4 ka = ld128(rW, ko, SC1), kb = ld128(rW, ko + 128 * RKS, SC1);
    if (jobF) {
      asm volatile("s_waitcnt vmcnt(2)" ::: "memory");
      const char* wp = lds + L_W + w * 4096 + h * 256 + r * 8;
#pragma unroll
      for (int i = 0; i < 8; ++i) Wf[i] = trfrag(wp + (i >> 2) * 16384 + (i & 3) * 1024, 128);
      PIN8(Wf);
    }
    asm volatile("" : "+v"(ka), "+v"(kb) :: "memory");
    u32 mx = max(umax4(ka), umax4(kb));
    RETRY(!__all(mx != SENT)) {
      FENCE();
      if (mx == SENT) { ka = ld128(rW, ko, SC1); kb = ld128(rW, ko + 128 * RKS, SC1); mx = max(umax4(ka), umax4(kb)); }
    }
    if (jobG) {
#pragma unroll
      for (int i = 8; i < 14; ++i) dma16(mkr(gwd, F * D * 2), wvo, i * 32 * 1024, ldsb + L_W + w * 14336 + i * 1024);
    }
    STAMP(12);
    u32 gm = max(mx, dppu<DPP_X1>(mx));
    gm = max(gm, dppu<DPP_X2>(gm)); gm = max(gm, dppu<DPP_HM>(gm)); gm = max(gm, dppu<DPP_M>(gm));
    if (r == 0) *(u32*)(lds + L_GM + (4 * w + h) * 4) = gm;
    const u32 ky[8] = {ka.x, ka.y, ka.z, ka.w, kb.x, kb.y, kb.z, kb.w};
    u32 m2 = 0;
#pragma unroll
    for (int e = 0; e < 8; ++e) m2 = max(m2, ky[e] == mx ? 0u : ky[e]);
    PIN(m2);
    bar();
    u32 mine;
    u32 rk = rank16(lds + L_GM, lane, mine);
    const u32 T = __builtin_amdgcn_readlane(mine, __builtin_ctzll(__ballot(rk == 7)));
    if (mx >= T) {
      const u32 pos = __hip_atomic_fetch_add((u32*)(lds + L_CNT), 1u, __ATOMIC_RELAXED, __HIP_MEMORY_SCOPE_WORKGROUP);
      *(u32*)(lds + L_LIST + pos * 4) = mx;
    }
    if (__any(m2 >= T)) {
#pragma unroll
      for (int e = 0; e < 8; ++e)
        if (ky[e] >= T && ky[e] != mx) {
          const u32 pos = __hip_atomic_fetch_add((u32*)(lds + L_CNT), 1u, __ATOMIC_RELAXED, __HIP_MEMORY_SCOPE_WORKGROUP);
          *(u32*)(lds + L_LIST + pos * 4) = ky[e];
        }
    }
    bar();
    u32 cand = *(const u32*)(lds + L_LIST + r * 4);
    const u32 N = peek(cnt, 0);
    if (N > 16) {
      for (u32 i = tid; i < N; i += 256) {
        const u32 kx = *(const u32*)(lds + L_LIST + i * 4);
        u32 c = 0;
#pragma unroll 1
        for (u32 q = 0; q < N; ++q) c += *(const u32*)(lds + L_LIST + q * 4) > kx;
        if (c < 8) *(u32*)(lds + L_SEL + c * 4) = kx;
      }
      if (arrive(cnt, 15) != 3) await<0>(cnt, 15, 4);
      if (tid < 16) *(u32*)(lds + L_LIST + tid * 4) = tid < 8 ? *(const u32*)(lds + L_SEL + tid * 4) : 0u;
      if (arrive(cnt, 15) != 7) await<0>(cnt, 15, 8);
      cand = *(const u32*)(lds + L_LIST + r * 4);
    }
    STAMP(13);
    if (jobF) {
      const int ho = OFF_H2 + (2047 - (int)(cand & 0x7ffu)) * 1024 + (w * 128 + 8 * h) * 2;
      u32x4 a[4];
#pragma unroll
      for (int ks = 0; ks < 4; ++ks) a[ks] = ld128(rW, ho + ks * 64, SC1);
      FENCE();
      rk = rank16(lds + L_LIST, lane, mine);
      if (w == 0 && lane < 16 && rk < 8) *(u32*)(lds + L_RANK + rk * 4) = (lane >> 2) * 256 + (lane & 3) * 4;
      RETRY(!__all(max(max(umax4(a[0]), umax4(a[1])), max(umax4(a[2]), umax4(a[3]))) != SENT)) {
        FENCE();
#pragma unroll
        for (int ks = 0; ks < 4; ++ks) a[ks] = ld128(rW, ho + ks * 64, SC1);
        PIN(a[0]); PIN(a[1]); PIN(a[2]); PIN(a[3]);
      }
      f32x4 ag = {0, 0, 0, 0}, au = {0, 0, 0, 0};
#pragma unroll
      for (int ks = 0; ks < 4; ++ks) { ag = mfma(a[ks], Wf[ks], ag); au = mfma(a[ks], Wf[4 + ks], au); }
      *(f32x4*)(lds + L_KC + w * 2048 + lane * 16) = ag;
      *(f32x4*)(lds + L_KC + w * 2048 + 1024 + lane * 16) = au;
      bar();
      STAMP(14);
      if (w < 2) {
        const char* p = lds + L_KC + *(const u32*)(lds + L_RANK + (lane >> 3) * 4) + (8 * w + (lane & 7)) * 16;
        float gv = 0, uv = 0;
#pragma unroll
        for (int i = 0; i < 4; ++i) { gv += *(const float*)(p + i * 2048); uv += *(const float*)(p + i * 2048 + 1024); }
        gv = rb(gv);
        const float v = rb(gv * __builtin_amdgcn_rcpf(1.f + __expf(-gv))) * rb(uv);
        const float vn = dppf<DPP_X1>(v);
        __builtin_amdgcn_raw_buffer_store_b32(nos(pk(v, vn)), rW, (lane & 1) ? OOB : OFF_GU + f0 * 16 + w * 128 + lane * 2, 0, SC1);
        STAMP(10);
      }
    } else {
      rk = rank16(lds + L_LIST, lane, mine);
      if (w == 0 && lane < 16 && rk < 8) *(u32*)(lds + L_SEL + rk * 4) = mine;
      bar();
      drain();
      {
        const char* wp = lds + L_W + w * 14336 + h * 256 + r * 8;
#pragma unroll
        for (int i = 0; i < 14; ++i) Wf[i] = trfrag(wp + i * 1024, 128);
        PIN8(Wf); PIN8(Wf + 6);
      }
      while (__builtin_amdgcn_s_memrealtime() < tb6 + GDELAY) __builtin_amdgcn_s_sleep(1);
      STAMP(14);
      const int go = OFF_GU + (w * 56 + 4 * (r >> 3) + h) * 128 + (r & 7) * 16;
      u32x4 a[7];
#pragma unroll 1
      for (int spins = 0; spins < SPIN_CAP; ++spins) {
        FENCE();
        u32 mm = 0;
#pragma unroll
        for (int i = 0; i < 7; ++i) a[i] = ld128(rW, go + i * 1024, SC1);
#pragma unroll
        for (int i = 0; i < 7; ++i) mm = max(mm, umax4(a[i]));
        if (__all(mm != SENT)) break;
      }
      STAMP(10);
      f32x4 a0 = {0, 0, 0, 0}, a1 = {0, 0, 0, 0};
#pragma unroll
      for (int i = 0; i < 7; ++i) { a0 = mfma(a[i], Wf[2 * i], a0); a1 = mfma(a[i], Wf[2 * i + 1], a1); }
      *(f32x4*)(lds + L_KC + w * 1024 + lane * 16) = lane < 32 ? a0 : a1;
      bar();
    }
    FLUSH();
  } else {
    const int a = w - 4, at = tid - 256, hl = lane & 31, row = 8 * j + 2 * a + (lane >> 5);
    __builtin_amdgcn_s_setprio(1);
    u32 cw = __builtin_amdgcn_raw_buffer_load_b32(mkr(gc, D * 2), jobM ? at * 4 : OOB, 0, 0);
    u32 dpair = __builtin_amdgcn_raw_buffer_load_b32(mkr(gssmD, D * 2), j * 4, 0, 0);
    FENCE();
    if (PF0SLP) __builtin_amdgcn_s_sleep(PF0SLP);
    u32x2 wrw = __builtin_amdgcn_raw_buffer_load_b64(mkr(gwr, D * 2), (at & 127) * 8, 0, 0);
    const u16* pP0 = jobG ? gwd : gwmod;
    PINS(pP0);
    const rsrc_t rP0 = mkr(pP0, D * 6 * D * 2);
    const int pf_g = m * 57344 + at * 128, pf_m = (m * 16 + (at >> 3)) * 6144 + g * 1024 + (at & 7) * 128;
    const int pf_n = jobG ? 192 : (g < 6 ? 128 : 0);
    int o_pf0 = at < pf_n ? (jobG ? pf_g : pf_m) : OOB;
    u32 pf0;
    if (PFPOS == 0) pf0 = __builtin_amdgcn_raw_buffer_load_b32(rP0, o_pf0, 0, NT);
    FENCE();
    EPOCH();
    MAKE_TAG();
    int o_own = j * 8192 + at * 16;
    int o_m = (a * 128 + (lane >> 2)) * 6144 + mcol * 2 + (lane & 3) * 16;
    int o_w = L_W + a * 8192 + ((lane >> 4) * 8 + ((lane & 15) >> 2)) * 64 + (lane & 3) * 8;
    asm volatile("" : "+v"(o_own), "+v"(o_m), "+v"(o_w), "+v"(s4), "+v"(o_pf0));
    publish_kv(Int<PFPOS == 0 ? 4 : 3>{}, [&]() __attribute__((always_inline)) {
      if (PFPOS == 1) { pf0 = __builtin_amdgcn_raw_buffer_load_b32(rP0, o_pf0, 0, NT); FENCE(); }
    });
    asm volatile("" : "+v"(dpair), "+v"(cw), "+v"(wrw) :: "memory");
    u32x4 av[4];
    auto mod_request = [&]() __attribute__((always_inline)) {
      const rsrc_t rM = mkr(gwmod, D * 6 * D * 2);
#pragma unroll
      for (int i = 0; i < 8; ++i) dma16(rM, o_m, i * 16 * 6144, ldsb + L_W + a * 8192 + i * 1024);
    };
    auto mod_compute = [&]() __attribute__((always_inline)) {
      STAMP(21);
      u32x4 bw[8];
#pragma unroll
      for (int i = 0; i < 8; ++i) bw[i] = trfrag(lds + o_w + (i >> 1) * 2048 + (i & 1) * 32, 256);
      PIN8(bw);
      f32x4 d0 = {0, 0, 0, 0}, d1 = {0, 0, 0, 0};
#pragma unroll
      for (int ks = 0; ks < 4; ++ks) { d0 = mfma(av[ks], bw[2 * ks], d0); d1 = mfma(av[ks], bw[2 * ks + 1], d1); }
      if (lane < 16) { *(float*)(lds + L_MP + a * 128 + lane * 4) = d0[0]; *(float*)(lds + L_MP + a * 128 + 64 + lane * 4) = d1[0]; }
      const u32 nth = arrive(cnt, 2);
      {
        f32x4 pp[4];
#pragma unroll
        for (int i = 0; i < 4; ++i) pp[i] = *(const f32x4*)(lds + L_MP + i * 128 + (lane & 7) * 16);
        float sm[4];
#pragma unroll
        for (int e = 0; e < 4; ++e) sm[e] = add1(add1(pp[0][e], pp[1][e]), add1(pp[2][e], pp[3][e]));
        const u32 p0 = pk(sm[0], sm[1]), p1 = pk(sm[2], sm[3]);
        st128((u32x4){p0, p1, p0 ^ tagv.x, p1 ^ tagv.y}, rW, nth == 3 ? OFF_MOD + (lane >> 3) * 12288 + mcol * 4 + (lane & 7) * 16 : OOB);
        FENCE();
      }
      STAMP(3);
    };
    if (jobM) {
      if (MPOS == 0) { await<1>(cnt, 12, 4); mod_request(); }
      f32x2 cv = up2(cw);
      cv.x = mul1(cv.x, __builtin_amdgcn_rcpf(add1(1.f, __builtin_amdgcn_exp2f(mul1(cv.x, -1.44269504088896f)))));
      cv.y = mul1(cv.y, __builtin_amdgcn_rcpf(add1(1.f, __builtin_amdgcn_exp2f(mul1(cv.y, -1.44269504088896f)))));
      *(u32*)(lds + L_SC + at * 4) = pk2(cv);
#pragma unroll
      for (int ks = 0; ks < 4; ++ks) av[ks] = *(const u32x4*)(lds + L_SC + a * 256 + ks * 64 + (lane >> 4) * 16);
      PIN(av[0]); PIN(av[1]); PIN(av[2]); PIN(av[3]);
      if (MPOS == 2) { await<1>(cnt, 12, 4); mod_request(); }
      if (MPOS == 0 || MPOS == 2) { drain(); mod_compute(); }
    }
    int o_sm = OOB, o_q = row * 1024 + hl * 16;
    if (at < 16) o_sm = OFF_RK + (at >> 1) * 256 * RKS + j * RKS + (at & 1) * 16;
    if (jobF && at >= 16 && at < 32) o_sm = OFF_GU + f0 * 16 + (at - 16) * 16;
    int o_pf = jobG ? m * 57344 + 24576 + at * 128 : (a < 2 ? (m * 16 + (lane >> 2)) * (F * 2) + g * 512 + (lane & 3) * 128 : OOB);
    const int un = 128 * a + 4 * (lane & 15) + (lane >> 4);
    int o_u = L_U + (30 + (un >> 1)) * 16 + ((un & 1) ? 0 : 8);
    int o_mod = OFF_MOD + g * 12288 + (a >> 1) * 6144 + (at & 127) * 16;
    asm volatile("" : "+v"(o_sm), "+v"(o_q), "+v"(o_pf), "+v"(o_u), "+v"(o_mod));
    await<1>(cnt, 3, 4);
    STAMP(6);
    PIN(pf0);
    if (MPOS == 1 && jobM) { mod_request(); if (arrive(cnt, 12) != 3) await<1>(cnt, 12, 4); }
    __builtin_amdgcn_raw_buffer_store_b128(s4, rW, o_own, OFF_H2, SC1);
    __builtin_amdgcn_raw_buffer_store_b128(s4, rW, o_own, OFF_H2 + 4096, SC1);
    st128(s4, rW, o_sm);
    FENCE();
    u32x4 q0 = ld128(mkr(gq, S * D * 2), o_q, NT), q1 = ld128(mkr(gq, S * D * 2), o_q + 512, NT);
    FENCE();
    u32 pf1 = __builtin_amdgcn_raw_buffer_load_b32(mkr(jobG ? gwd : (a == 0 ? gwg : gwu), D * F * 2), o_pf, 0, NT);
    FENCE();
    {
      const f32x2 dv = up2(dpair);
#pragma unroll
      for (int t = 0; t < 2; ++t) {
        const u32x2 ua = *(const u32x2*)(lds + o_u + 512 * t), ub = *(const u32x2*)(lds + o_u + 512 * t + 4608);
        char* dp = lds + L_KD + (2 * a + t) * 1024 + lane * 16;
        *(f32x4*)(dp) = (f32x4){mul0(hi2f(ua.y), dv.x), mul0(lo2f(ua.y), dv.x), mul0(hi2f(ua.x), dv.x), mul0(lo2f(ua.x), dv.x)};
        *(f32x4*)(dp + 8192) = (f32x4){mul0(hi2f(ub.y), dv.y), mul0(lo2f(ub.y), dv.y), mul0(hi2f(ub.x), dv.y), mul0(lo2f(ub.x), dv.y)};
      }
    }
    if (MPOS == 1 && jobM) {
      asm volatile("s_waitcnt vmcnt(6)" ::: "memory");
      mod_compute();
      asm volatile("s_waitcnt vmcnt(4)" ::: "memory");
    } else {
      asm volatile("s_waitcnt vmcnt(3)" ::: "memory");
    }
    signal(cnt, 14);
    STAMP(5);
    asm volatile("" : "+v"(q0), "+v"(q1) :: "memory");
    STAMP(7);
    __builtin_amdgcn_s_setprio(0);
    f32x2 x[8], gah[8];
    {
      unpack16(q0, q1, x);
      float s = 0;
#pragma unroll
      for (int e = 0; e < 8; ++e) s = add0(s, add0(x[e].x, x[e].y));
      const float mean = half_sum(s) * (1.f / 512.f);
      float qq = 0;
#pragma unroll
      for (int e = 0; e < 8; ++e) { const float d0 = add0(x[e].x, -mean), d1 = add0(x[e].y, -mean); qq = fma0(d0, d0, qq); qq = fma0(d1, d1, qq); }
      const float var = rb(half_sum(qq) * (1.f / 512.f));
      float isd = __builtin_amdgcn_rcpf(rb(__builtin_amdgcn_sqrtf(rb(var + 1e-5f))));
      float mi = -rb(mean) * isd;
      PIN(isd); PIN(mi);
#pragma unroll
      for (int e = 0; e < 8; ++e) gah[e] = (f32x2){fma0(x[e].x, isd, mi), fma0(x[e].y, isd, mi)};
      PIN8(x); PIN8(gah);
    }
    STAMP(8);
    asm volatile("" : "+v"(pf1) :: "memory");
    if (F1POS == 1 && jobF) {
#pragma unroll
      for (int i = 0; i < 4; ++i) dma16(mkr(gwg, D * F * 2), (a * 128 + (lane >> 1)) * (F * 2) + f0 * 2 + (lane & 1) * 16, i * 32 * F * 2, ldsb + L_W + a * 4096 + i * 1024);
    }
    STAMP(4);
    if (MRD == 0) await<1>(cnt, 9, 4);
    if (MODSLP) __builtin_amdgcn_s_sleep(MODSLP);
    {
      u32x4 mu[3];
#pragma unroll
      for (int i = 0; i < 3; ++i) mu[i] = ld128(rW, o_mod + i * 2048, SC1);
      auto modbad = [&]() __attribute__((always_inline)) {
        u32 d = 0;
#pragma unroll
        for (int i = 0; i < 3; ++i) d |= (mu[i].x ^ mu[i].z ^ tagv.x) | (mu[i].y ^ mu[i].w ^ tagv.y);
        return d != 0;
      };
      bool nb = modbad();
      RETRY(__any(nb)) {
        FENCE();
        __builtin_amdgcn_s_sleep(3);
        if (nb) {
#pragma unroll
          for (int i = 0; i < 3; ++i) mu[i] = ld128(rW, o_mod + i * 2048, SC1);
        }
        asm volatile("" : "+v"(mu[0]), "+v"(mu[1]), "+v"(mu[2]));
        nb = modbad();
      }
      __builtin_amdgcn_s_setprio(1);
      STAMP(17);
      const f32x2 s0 = up2(mu[0].x), s1 = up2(mu[0].y), c0 = rb2(up2(mu[1].x) + 1.f), c1 = rb2(up2(mu[1].y) + 1.f), g0 = up2(mu[2].x), g1 = up2(mu[2].y);
      char* mo = lds + L_MOD + (at & 1) * 1024 + ((at & 127) >> 1) * 16;
      auto put = [&](int v, f32x2 lo, f32x2 hi) __attribute__((always_inline)) { *(f32x4*)(mo + v * 2048) = (f32x4){lo.x, lo.y, hi.x, hi.y}; };
      if (a < 2) {
        put(0, g0 * c0, g1 * c1);
        put(1, g0 * s0, g1 * s1);
      } else {
        const f32x2 w0 = up2(wrw.x), w1 = up2(wrw.y), r0 = c0 * w0, r1 = c1 * w1, sa_ = r0 + r1, sc_ = s0 * w0 + s1 * w1;
        put(2, r0, r1); put(3, c0, c1); put(4, s0, s1); put(5, g0, g1);
        const float pa = half_sum(sa_.x + sa_.y), pc2 = half_sum(sc_.x + sc_.y);
        if (hl == 0) *(f32x2*)(lds + L_MOD + 12288 + ((a & 1) * 2 + (lane >> 5)) * 8) = (f32x2){pa, pc2};
      }
      STAMP(9);
      if (arrive(cnt, 13) != 3) await<1>(cnt, 13, 4);
    }
    f32x2 ra[8], cb1[8], sb[8], gb[8];
    float suma, sumc;
    {
      const char* mp = lds + L_MOD + hl * 16;
      auto vec16 = [&](int v, f32x2* o) __attribute__((always_inline)) {
#pragma unroll
        for (int i = 0; i < 4; ++i) {
          const f32x4 t = *(const f32x4*)(mp + v * 2048 + (i >> 1) * 512 + (i & 1) * 1024);
          o[2 * i] = (f32x2){t[0], t[1]}; o[2 * i + 1] = (f32x2){t[2], t[3]};
        }
      };
      f32x2 a1[8], a0[8];
      vec16(0, a1); vec16(1, a0); vec16(2, ra); vec16(3, cb1); vec16(4, sb); vec16(5, gb);
      const f32x4 pa = *(const f32x4*)(lds + L_MOD + 12288), pc2 = *(const f32x4*)(lds + L_MOD + 12288 + 16);
#pragma unroll
      for (int e = 0; e < 8; ++e) gah[e] = gah[e] * a1[e] + a0[e];
      suma = (pa[0] + pa[2]) + (pc2[0] + pc2[2]);
      sumc = (pa[1] + pa[3]) + (pc2[1] + pc2[3]);
      PIN8(gah); PIN8(ra); PIN8(cb1); PIN8(sb); PIN8(gb); PIN(suma); PIN(sumc);
    }
    int o_key = hl < 8 ? OFF_RK + hl * 256 * RKS + j * RKS + (row & 7) * 4 : OOB;
    PIN(o_key);
    STAMP(10);
    bar();
    STAMP(11);
    {
      f32x2 z[8];
      const char* zr = lds + L_Z + (2 * a + (lane >> 5)) * 1040 + hl * 16;
      unpack16(*(const u32x4*)(zr), *(const u32x4*)(zr + 512), z);
      f32x2 s1 = {0, 0}, s2 = {0, 0}, s3 = {0, 0};
#pragma unroll
      for (int e = 0; e < 8; ++e) { x[e] += gah[e] * z[e]; s1 += x[e]; s2 += x[e] * x[e]; s3 += x[e] * ra[e]; }
      const float t1 = half_sum(s1.x + s1.y), t2 = half_sum(s2.x + s2.y), t3 = half_sum(s3.x + s3.y);
      const float mean = t1 * (1.f / 512.f);
      const f32x2 mvb = rb2((f32x2){mean, fmaxf(t2 * (1.f / 512.f) - mean * mean, 0.f)});
      const float mb = mvb.x, isd = __builtin_amdgcn_rcpf(rb(__builtin_amdgcn_sqrtf(rb(mvb.y + 1e-5f))));
      const float rr = (t3 - mb * suma) * isd + sumc;
      u32 kb = pk(0.f, rr);
      kb ^= ((int)kb < 0) ? 0xffff0000u : 0x80000000u;
      __builtin_amdgcn_raw_buffer_store_b32(kb | 0x8000u | (u32)(2047 - row), rW, o_key, 0, SC1);
      signal(cnt, 10);
      STAMP(12);
      u32 hw[8], ow[8];
#pragma unroll
      for (int e = 0; e < 8; ++e) {
        const f32x2 h2 = (x[e] - mb) * isd * cb1[e] + sb[e];
        hw[e] = nos(pk2(h2));
        ow[e] = pk2(x[e] + gb[e] * h2);
      }
      PIN8(hw); PIN8(ow);
      drain();
      bar();
      STAMP(13);
      st128((u32x4){hw[0], hw[1], hw[2], hw[3]}, rW, OFF_H2 + row * 1024 + hl * 16);
      st128((u32x4){hw[4], hw[5], hw[6], hw[7]}, rW, OFF_H2 + row * 1024 + 512 + hl * 16);
      st128((u32x4){ow[0], ow[1], ow[2], ow[3]}, rO, row * 1024 + hl * 16);
      st128((u32x4){ow[4], ow[5], ow[6], ow[7]}, rO, row * 1024 + 512 + hl * 16);
    }
    if (b == 0 && a == 0) __builtin_amdgcn_raw_buffer_store_b64(tagv, rW, lane == 0 ? OFF_EP : OOB, 0, SC1);
    if (jobF || jobG) { bar(); bar(); }
    drain();
    STAMP(14);
    if (arrive(cnt, 5) == 3) __builtin_amdgcn_raw_buffer_store_b64(tagv, rW, lane == 0 ? OFF_FL3 + j * 128 : OOB, 0, SC1);
    if (jobG && a < 2) {
      __builtin_amdgcn_s_setprio(0);
      bar();
      const int r = lane & 15, tok = at >> 4;
      const u32 key = *(const u32*)(lds + L_SEL + tok * 4);
      const int orow = 2047 - (int)(key & 0x7ffu);
#pragma unroll 1
      for (int spins = 0; spins < SPIN_CAP; ++spins) {
        FENCE();
        const u32x2 f = __builtin_amdgcn_raw_buffer_load_b64(rW, OFF_FL3 + (orow >> 3) * 128, 0, SC1);
        if (__all(f.x == tagv.x && f.y == tagv.y)) break;
        __builtin_amdgcn_s_sleep(4);
      }
      FENCE();
      float oold = lo2f(__builtin_amdgcn_raw_buffer_load_b16(rO, orow * 1024 + (gd0 + r) * 2, 0, SC1));
      u32 rbits = key >> 16;
      rbits = (rbits & 0x8000u) ? (rbits & 0x7fffu) : (rbits ^ 0xffffu);
      float gy = rb(__builtin_amdgcn_rcpf(1.f + __expf(-lo2f(rbits)))) * *(const float*)(lds + L_MOD + 10240 + ((r >> 2) & 1) * 1024 + ((gd0 + r) >> 3) * 16 + (r & 3) * 4);
      int oo = (r & 1) ? OOB : orow * 1024 + (gd0 + r) * 2, po = L_KC + ((tok >> 2) * 16 + r) * 16 + (tok & 3) * 4;
      PIN(gy); PIN(oold); PIN(oo); PIN(po);
      STAMP(15);
      bar();
      float y = 0;
#pragma unroll
      for (int i = 0; i < 8; ++i) y += *(const float*)(lds + po + i * 512);
      const float o = oold + gy * rb(y);
      const float on = dppf<DPP_X1>(o);
      __builtin_amdgcn_raw_buffer_store_b32(pk(o, on), rO, oo, 0, SC1);
      STAMP(30);
      { const u64 until = __builtin_amdgcn_s_memrealtime() + 4 * (tprobe - tstart); while (__builtin_amdgcn_s_memrealtime() < until) __builtin_amdgcn_s_sleep(2); }
    }
    FLUSH();
  }
}

extern "C" void kernel_launch(void* const* d_in, const int* in_sizes, int n_in, void* d_out, int out_size, void* d_ws,
                              size_t ws_size, hipStream_t stream) {
  fused_kernel<<<256, 512, 0, stream>>>((char*)d_ws + (WSEND ? ((ws_size - WS_BYTES) & ~(size_t)4095) : 0), desc_hi(WS_BYTES), (const u16*)d_in[1], desc_hi(S * D * 2), (const u16*)d_in[2],
                                        desc_hi(S * D * 2), (const u16*)d_in[3], desc_hi(D * 8), (const u16*)d_in[4], (const u16*)d_in[11],
                                        (const u16*)d_in[12], (const u16*)d_in[0], (const u16*)d_in[5], (const u16*)d_in[7],
                                        (const u16*)d_in[8], (const u16*)d_in[9], (const u16*)d_in[10], (u16*)d_out);
}
```

```cpp
#define PST 14
#define PW 4
#define PK 0
#include <hip/hip_runtime.h>
#include <stdint.h>

typedef unsigned short u16;
typedef unsigned int u32;
typedef unsigned long long u64;
typedef short bf16x8 __attribute__((ext_vector_type(8)));
typedef short s16x4 __attribute__((ext_vector_type(4)));
typedef float f32x4 __attribute__((ext_vector_type(4)));
typedef float f32x2 __attribute__((ext_vector_type(2)));
typedef __bf16 bf16x2 __attribute__((ext_vector_type(2)));
typedef u32 u32x4 __attribute__((ext_vector_type(4)));
typedef u32 u32x2 __attribute__((ext_vector_type(2)));
typedef __amdgpu_buffer_rsrc_t rsrc_t;
#define LAS __attribute__((address_space(3)))

constexpr int S = 2048, D = 512, F = 1792;
constexpr u32 SENT = 0xFFFFFFFFu;
constexpr int SC1 = 16, NT = 2;
constexpr int SPIN_CAP = 1 << 14;
#define RKS 32
#define JOBPOS 0
#define FW0 4
#define CHKSLP 4
#define ZSLP 0
#define MPOS 0
#define E1SLP 0
#define MRD 1
#define PFPOS 0
#define F1POS 0
#define WSEND 0
#define KSLP 0
#define MODSLP 0
#define F2S 8
#define SSMPOS 0
#define F1S 8
#define PF0SLP 0
#define GDELAY 200

constexpr int OFF_E1 = 0;
constexpr int OFF_E2 = 2 << 20;
constexpr int OFF_H2 = 4 << 20;
constexpr int OFF_SM = 6 << 20;
constexpr int OFF_MOD = OFF_SM + 524288;
constexpr int OFF_RK = 7 << 20;
constexpr int OFF_GU = OFF_SM + 65536;
constexpr int OFF_CK = OFF_SM + 262144;
constexpr int OFF_CZ = (7 << 20) + 524288;
constexpr int OFF_EP = OFF_SM + 131072;
constexpr int OFF_FL3 = OFF_SM + 196608;
constexpr int OFF_ST = 8 << 20;
constexpr int WS_BYTES = 16 << 20;
constexpr int OOB = 0x7ffffff0;

constexpr int KC_STRIDE = 4160;
constexpr int L_KC = 0;
constexpr int L_U = 33280;
constexpr int L_Z = L_U + 9216;
constexpr int L_W = L_Z + 8320;
constexpr int L_KV = L_W + 57344;
constexpr int L_KD = L_KV;
constexpr int L_SC = L_KV + 18432;
constexpr int L_MOD = L_SC + 1024;
constexpr int L_MP = L_MOD + 12288 + 32;
constexpr int L_GM = L_MP + 512;
constexpr int L_CNT = L_GM + 64;
constexpr int L_RANK = L_CNT + 64;
constexpr int L_SEL = L_RANK + 64;
constexpr int L_LIST = L_SEL + 32;
constexpr int L_END = L_LIST + 4096;

#define STAMP(i) do { if ((i) == PST && (w == PW)) { const u64 tn_ = __builtin_amdgcn_s_memrealtime(); __builtin_amdgcn_raw_buffer_store_b128((u32x4){(u32)tstart, (u32)(tstart >> 32), (u32)tn_, (u32)(tn_ >> 32)}, mkr(wsp, WS_BYTES), lane == 0 ? OFF_ST + b * 16 : OOB, 0, SC1); asm volatile("s_waitcnt vmcnt(0)" ::: "memory"); } } while (0)
#define FLUSH() do { } while (0)

template <int N> struct Int { static constexpr int value = N; };
#define PIN(x) asm volatile("" : "+v"(x))
#define PINS(x) asm volatile("" : "+s"(x))
#define PIN8(a) asm volatile("" : "+v"((a)[0]), "+v"((a)[1]), "+v"((a)[2]), "+v"((a)[3]), "+v"((a)[4]), "+v"((a)[5]), "+v"((a)[6]), "+v"((a)[7]))
#define FENCE() asm volatile("" ::: "memory")
#define RETRY(bad) if (__builtin_expect(bad, 0)) _Pragma("unroll 1") for (int spins = 0; spins < SPIN_CAP && (spins == 0 || (bad)); ++spins)
#define SCHED() __builtin_amdgcn_sched_barrier(0)
__device__ __forceinline__ float lo2f(u32 w) { return __uint_as_float(w << 16); }
__device__ __forceinline__ float hi2f(u32 w) { return __uint_as_float(w & 0xffff0000u); }
__device__ __forceinline__ f32x2 up2(u32 w) { return (f32x2){lo2f(w), hi2f(w)}; }
__device__ __forceinline__ u32 pk2(f32x2 v) { return __builtin_bit_cast(u32, __builtin_convertvector(v, bf16x2)); }
__device__ __forceinline__ u32 pk(float lo, float hi) { return pk2((f32x2){lo, hi}); }
__device__ __forceinline__ u32 nos(u32 x) { return min(x, SENT - 1u); }
__device__ __forceinline__ float rb(float x) { return (float)(__bf16)x; }
__device__ __forceinline__ f32x2 rb2(f32x2 v) { return up2(pk2(v)); }
__device__ __forceinline__ float mul1(float a, float b) { float r; asm("s_nop 0\n\tv_mul_f32 %0, %1, %2" : "=v"(r) : "v"(a), "v"(b)); return r; }
__device__ __forceinline__ float add1(float a, float b) { float r; asm("s_nop 0\n\tv_add_f32 %0, %1, %2" : "=v"(r) : "v"(a), "v"(b)); return r; }
__device__ __forceinline__ float mul0(float a, float b) { float r; asm("v_mul_f32 %0, %1, %2" : "=v"(r) : "v"(a), "v"(b)); return r; }
__device__ __forceinline__ float add0(float a, float b) { float r; asm("v_add_f32 %0, %1, %2" : "=v"(r) : "v"(a), "v"(b)); return r; }
__device__ __forceinline__ float fma0(float a, float b, float c) { float r; asm("v_fma_f32 %0, %1, %2, %3" : "=v"(r) : "v"(a), "v"(b), "v"(c)); return r; }
__device__ __forceinline__ u32 fold2(u32 a, u32 b) { return a + __builtin_amdgcn_alignbit(b, b, 21); }
__device__ __forceinline__ u32 umax4(u32x4 x) { return max(max(x.x, x.y), max(x.z, x.w)); }
__device__ __forceinline__ void bar() { asm volatile("s_waitcnt lgkmcnt(0)\n\ts_barrier" ::: "memory"); }
__device__ __forceinline__ void drain() { asm volatile("s_waitcnt vmcnt(0)" ::: "memory"); }
__device__ __forceinline__ rsrc_t mkr(const void* p, int bytes) { return __builtin_amdgcn_make_buffer_rsrc((void*)p, 0, bytes, 0x00020000); }
__device__ __forceinline__ u32x4 ld128(rsrc_t r, int off, int aux) {
  return aux == SC1 ? __builtin_amdgcn_raw_buffer_load_b128(r, off, 0, SC1)
                    : (aux == NT ? __builtin_amdgcn_raw_buffer_load_b128(r, off, 0, NT) : __builtin_amdgcn_raw_buffer_load_b128(r, off, 0, 0));
}
__device__ __forceinline__ void st128(u32x4 v, rsrc_t r, int off) { __builtin_amdgcn_raw_buffer_store_b128(v, r, off, 0, SC1); }
__device__ __forceinline__ f32x4 mfma(u32x4 a, u32x4 b, f32x4 c) {
  return __builtin_amdgcn_mfma_f32_16x16x32_bf16(__builtin_bit_cast(bf16x8, a), __builtin_bit_cast(bf16x8, b), c, 0, 0, 0);
}
__device__ __forceinline__ void dma16(rsrc_t r, int voff, int soff, u32 ldsaddr) {
  asm volatile("s_mov_b32 m0, %2\n\ts_nop 0\n\tbuffer_load_dwordx4 %0, %1, %3 offen nt lds" :: "v"(voff), "s"(r), "s"(ldsaddr), "s"(soff) : "memory");
}
__device__ __forceinline__ u32x2 trread(const char* p) {
  return __builtin_bit_cast(u32x2, __builtin_amdgcn_ds_read_tr16_b64_v4i16((LAS s16x4*)(LAS char*)p));
}
__device__ __forceinline__ float dot2(u32 a, u32 b, float c) {
  return __builtin_amdgcn_fdot2_f32_bf16(__builtin_bit_cast(bf16x2, a), __builtin_bit_cast(bf16x2, b), c, false);
}
__device__ __forceinline__ void dma16_sc1(rsrc_t r, int voff, int soff, u32 ldsaddr) {
  asm volatile("s_mov_b32 m0, %2\n\ts_nop 0\n\tbuffer_load_dwordx4 %0, %1, %3 offen sc1 lds" :: "v"(voff), "s"(r), "s"(ldsaddr), "s"(soff) : "memory");
}
__device__ __forceinline__ u32x4 trfrag(const char* p, int q4) {
  const u32x2 a = __builtin_bit_cast(u32x2, __builtin_amdgcn_ds_read_tr16_b64_v4i16((LAS s16x4*)(LAS char*)p));
  const u32x2 c = __builtin_bit_cast(u32x2, __builtin_amdgcn_ds_read_tr16_b64_v4i16((LAS s16x4*)(LAS char*)(p + q4)));
  return (u32x4){a.x, a.y, c.x, c.y};
}
template <int CTRL> __device__ __forceinline__ u32 dppu(u32 x) { return (u32)__builtin_amdgcn_mov_dpp((int)x, CTRL, 0xf, 0xf, true); }
template <int CTRL> __device__ __forceinline__ float dppf(float x) { return __uint_as_float(dppu<CTRL>(__float_as_uint(x))); }
constexpr int DPP_X1 = 0xB1, DPP_X2 = 0x4E, DPP_HM = 0x141, DPP_M = 0x140;
__device__ __forceinline__ float half_sum(float v) {
  v += dppf<DPP_X1>(v); v += dppf<DPP_X2>(v); v += dppf<DPP_HM>(v); v += dppf<DPP_M>(v);
  auto s = __builtin_amdgcn_permlane16_swap(__float_as_uint(v), __float_as_uint(v), false, false);
  return __uint_as_float(s[0]) + __uint_as_float(s[1]);
}
__device__ __forceinline__ void unpack16(u32x4 a, u32x4 c, f32x2* o) {
#pragma unroll
  for (int e = 0; e < 4; ++e) { o[e] = up2(a[e]); o[4 + e] = up2(c[e]); }
}
__device__ __forceinline__ u32 rank16(const char* lds, int lane, u32& mine) {
  mine = *(const u32*)(lds + (lane & 15) * 4);
  const u32x4 o = *(const u32x4*)(lds + (lane >> 4) * 16);
  u32 c = (o.x > mine) + (o.y > mine) + (o.z > mine) + (o.w > mine);
  auto s = __builtin_amdgcn_permlane16_swap(c, c, false, false);
  c = s[0] + s[1];
  auto t = __builtin_amdgcn_permlane32_swap(c, c, false, false);
  return t[0] + t[1];
}
__device__ __forceinline__ u32 arrive(u32 cnt, int idx) {
  u32 old, tmp;
  asm volatile("s_waitcnt lgkmcnt(0)\n\ts_mov_b64 exec, 1\n\tv_mov_b32 %1, 1\n\tds_add_rtn_u32 %1, %2, %1 offset:%3\n\ts_mov_b64 exec, -1\n\t"
               "s_waitcnt lgkmcnt(0)\n\tv_readfirstlane_b32 %0, %1" : "=s"(old), "=&v"(tmp) : "v"(cnt), "i"(idx * 4) : "memory");
  return old;
}
__device__ __forceinline__ void signal(u32 cnt, int idx) {
  u32 tmp;
  asm volatile("s_waitcnt lgkmcnt(0)\n\ts_mov_b64 exec, 1\n\tv_mov_b32 %0, 1\n\tds_add_u32 %1, %0 offset:%2\n\ts_mov_b64 exec, -1"
               : "=&v"(tmp) : "v"(cnt), "i"(idx * 4) : "memory");
}
__device__ __forceinline__ u32 peek(u32 cnt, int idx) {
  u32 v, tmp;
  asm volatile("ds_read_b32 %1, %2 offset:%3\n\ts_waitcnt lgkmcnt(0)\n\tv_readfirstlane_b32 %0, %1" : "=s"(v), "=&v"(tmp) : "v"(cnt), "i"(idx * 4) : "memory");
  return v;
}
template <int SLEEP> __device__ __forceinline__ void await(u32 cnt, int idx, u32 n) {
#pragma unroll 1
  for (int spins = 0; peek(cnt, idx) < n && spins < (SPIN_CAP << 6); ++spins) if (SLEEP) __builtin_amdgcn_s_sleep(SLEEP);
}

typedef u32 u32x16 __attribute__((ext_vector_type(16)));
typedef u64 u64x8 __attribute__((ext_vector_type(8)));
typedef u64 u64x2 __attribute__((ext_vector_type(2)));
constexpr u64 desc_hi(u32 bytes) { return (u64)bytes | ((u64)0x00020000u << 32); }
#define SUB4(v, i) __builtin_shufflevector(v, v, 4 * (i), 4 * (i) + 1, 4 * (i) + 2, 4 * (i) + 3)

__global__ __launch_bounds__(512, 2) void fused_kernel(
    char* a0, u64 a1, const u16* a2, u64 a3, const u16* a4, u64 a5, const u16* a6, u64 a7, const u16* a8, const u16* a9,
    const u16* a10, const u16* a11, const u16* a12, const u16* a13, const u16* a14, const u16* a15, const u16* a16, u16* a17) {
  __shared__ __attribute__((aligned(16))) char lds[L_END];
  u32x16 ka0; u64x8 ka1; u64x2 ka2;
  asm volatile("s_load_dwordx16 %0, %3, 0x0\n\ts_load_dwordx16 %1, %3, 0x40\n\ts_load_dwordx4 %2, %3, 0x80"
               : "=&s"(ka0), "=&s"(ka1), "=&s"(ka2) : "s"(__builtin_amdgcn_kernarg_segment_ptr()));
  SCHED();
  const u64 tstart = __builtin_amdgcn_s_memrealtime();
  u64 tprobe = tstart;
  char* wsp = nullptr;
  const int tid = threadIdx.x, lane = tid & 63;
  const int w = __builtin_amdgcn_readfirstlane(tid >> 6);
  const int b = blockIdx.x, g = b & 7, m = b >> 3, j = 32 * g + m, cg = (g + 1) & 7;
  u32x4 s4 = {SENT, SENT, SENT, SENT};

  const u32 ldsb = (u32)(size_t)(LAS char*)lds;
  int kvo = ((tid >> 3) << 10) | ((((tid & 7) ^ ((tid >> 3) & 2) ^ ((tid >> 4) & 4)) ^ 4) << 4);
  u32 cls = OFF_E1 + cg * 262144 + m * 256, kvs = m * 65536 + cg * 128, kml = ldsb + L_KV + 1024 + w * 1024, vml = kml + 8192;
  int kho = (lane >= 40 && m > 0) ? ((kvo & 0x1fff) ^ 64) : OOB;
  int sko = lane < 32 ? lane * 16 : OOB;
  asm volatile("" : "+v"(kvo), "+v"(kho), "+v"(sko));
  asm volatile("" : "+s"(kvs), "+s"(kml), "+s"(vml));
  SCHED();
  asm volatile("s_waitcnt lgkmcnt(0)" : "+s"(ka0), "+s"(ka1), "+s"(ka2));
  asm volatile("s_mov_b32 m0, %3\n\ts_nop 0\n\tbuffer_load_dwordx4 %0, %1, %2 offen nt lds\n\t"
               "s_mov_b32 m0, %5\n\ts_nop 0\n\tbuffer_load_dwordx4 %0, %4, %2 offen nt lds"
               :: "v"(kvo), "s"(SUB4(ka0, 1)), "s"(kvs), "s"(kml), "s"(SUB4(ka0, 2)), "s"(vml) : "memory");
  if (w == 4)
    asm volatile("s_mov_b32 m0, %3\n\ts_nop 0\n\tbuffer_load_dwordx4 %0, %1, %2 offen nt lds"
                 :: "v"(kho), "s"(SUB4(ka0, 1)), "s"(m > 0 ? kvs - 8192 : 0u), "s"(ldsb + L_KV) : "memory");
  if (w == 5)
    asm volatile("s_mov_b32 m0, %3\n\ts_nop 0\n\tbuffer_load_dwordx4 %0, %1, %2 offen lds"
                 :: "v"(sko), "s"(SUB4(ka0, 3)), "s"(cg * 512), "s"(ldsb + L_KV + 17408) : "memory");
  SCHED();
  char* ws = (char*)(((u64)ka0[1] << 32) | ka0[0]);
  const u16 *gssmk = (const u16*)ka1[0], *gc = (const u16*)ka1[1], *gwmod = (const u16*)ka1[2], *gq = (const u16*)ka1[3],
            *gssmD = (const u16*)ka1[4], *gwr = (const u16*)ka1[5], *gwg = (const u16*)ka1[6], *gwu = (const u16*)ka1[7], *gwd = (const u16*)ka2[0];
  u16* gout = (u16*)ka2[1];
  wsp = ws;
  const rsrc_t rW = mkr(ws, WS_BYTES);
  const bool jobG = g == 7, jobF = g < 7 && m >= 16, jobM = g < 6 && m < 16;
  const int mcol = g * 512 + m * 32;
  int e1_out = cls + ((w & 3) * 8 + ((lane & 15) >> 1)) * 8192 + (w >> 2) * 128 + (lane & 1) * 64 + (lane >> 4) * 16;
  STAMP(16);
  if (w >= 6) {
    const int at = tid - 384;
    int zo = L_CNT + (at & 3) * 16;
    if (at < 60) zo = L_U + (at >= 30 ? 4608 - 480 : 0) + at * 16;
    if (at >= 64 && at < 80) zo = L_KC + ((at >> 1) & 7) * KC_STRIDE + (at & 1) * 16;
    if (at >= 80 && at < 84) zo = L_LIST + (at & 3) * 16;
    *(u32x4*)(lds + zo) = (u32x4){0, 0, 0, 0};
  }
  const int f0 = g * 256 + (m - 16) * 16;
  const int gd0 = m * 16;
  u32 cnt = ldsb + L_CNT;
  PIN(cnt);
  u64 ep;
#define EPOCH() asm volatile("s_load_dwordx2 %0, %1, %2\n\ts_waitcnt lgkmcnt(0)" : "=&s"(ep) : "s"(ws), "s"(OFF_EP) : "memory"); STAMP(18)
  u32x2 tagv;
#define MAKE_TAG() do { const u64 tag = ep + 1ull; tagv = (u32x2){(u32)tag, (u32)(tag >> 32)}; } while (0)
  auto publish_kv = [&](auto younger, auto hook) __attribute__((always_inline)) {
    const int blk = 4 * (w >> 2) + (lane >> 4) + 1;
    int ko = (blk * 8 + ((lane & 15) >> 2)) * 128 + (((w & 3) ^ ((lane >> 3) & 1) ^ ((blk & 1) << 1)) << 5) + (lane & 3) * 8;
    int cko = (lane & 17) == 0 ? OFF_CK + ((cg * 4 + (w & 3)) * 64 + m * 2 + (w >> 2)) * 64 + (lane & 14) * 4 + (lane >> 5) * 4 : OOB;
    u32 tw = lane < 32 ? tagv.x : tagv.y;
    PIN(ko); PIN(e1_out); PIN(cko); PIN(tw);
    int sko2 = L_KV + 17408 + ((w & 3) * 16 + (lane & 15)) * 8, ko2 = L_KV - 512 + (ko ^ 64);
    PIN(sko2); PIN(ko2);
    asm volatile("s_waitcnt vmcnt(%0)\n\ts_barrier" :: "i"(younger.value) : "memory");
    STAMP(20);
    hook();
    const char* kp = lds + L_KV + ko;
    const u32x2 sk = *(const u32x2*)(lds + sko2);
    const u32x2 p0 = trread(lds + ko2), p1 = trread(kp), p2 = trread(kp + 512), v0 = trread(kp + 8192), v1 = trread(kp + 8704);
    const u32 c10 = __builtin_amdgcn_alignbyte(sk.x, sk.x, 2), c32 = __builtin_amdgcn_alignbyte(sk.y, sk.y, 2);
    const u32 c0z = sk.x & 0xffffu, cz3 = sk.y & 0xffff0000u, c21 = (sk.y & 0xffffu) | (sk.x & 0xffff0000u);
    const u32 P[6] = {p0.x, p0.y, p1.x, p1.y, p2.x, p2.y}, V[4] = {v0.x, v0.y, v1.x, v1.y};
    u32 o[4];
#pragma unroll
    for (int i = 0; i < 4; ++i) {
      const float ev = dot2(P[i + 2], c0z, dot2(P[i + 1], c21, dot2(P[i], cz3, 0.f)));
      const float od = dot2(P[i + 2], c10, dot2(P[i + 1], c32, 0.f));
      o[i] = pk(od * hi2f(V[i]), ev * lo2f(V[i]));
    }
    st128((u32x4){o[3], o[2], o[1], o[0]}, rW, e1_out);
    FENCE();
    {
      auto t = __builtin_amdgcn_permlane32_swap(fold2(o[3], o[2]), fold2(o[1], o[0]), false, false);
      u32 hs = t[0] + t[1];
      auto u = __builtin_amdgcn_permlane16_swap(hs, hs, false, false);
      hs = u[0] + u[1];
      hs += dppu<DPP_X1>(hs);
      __builtin_amdgcn_raw_buffer_store_b32(hs + tw, rW, cko, 0, SC1);
    }
    FENCE();
    STAMP(2);
  };
  const rsrc_t rO = mkr(gout, S * D * 2);

  if (w < 4) {
    const int ch = w >> 1, half = w & 1, r = lane & 15, h = lane >> 4;
    u32x2 kx;
    auto taps = [&]() __attribute__((always_inline)) {
      const rsrc_t rS = mkr(gssmk, S * D * 2);
      const u32 dst = ldsb + L_KC + ch * (4 * KC_STRIDE) + 32 + half * 2048;
      dma16(rS, lane * 16, j * 8192 + w * 2048, dst);
      dma16(rS, lane * 16, j * 8192 + w * 2048 + 1024, dst + 1024);
      kx = __builtin_amdgcn_raw_buffer_load_b64(rS, j * 8192 + w * 2048 + 2048, 0, NT);
      FENCE();
    };
    if (SSMPOS == 0) taps();
    EPOCH();
    MAKE_TAG();
    int cki = OFF_CK + ((g * 4 + (m >> 3)) * 64 + w * 8 + (lane >> 3)) * 64 + (m & 7) * 8, e1i = OFF_E1 + j * 8192 + tid * 16;
    int o_z = OFF_E2 + j * 8192 + (128 * w + 4 * r + h) * 16;
    int o_cz = OFF_CZ + j * 2048 + w * 512 + r * 16 + h * 4;
    u32 tw2 = (h & 1) ? tagv.y : tagv.x;
    PIN(cki); PIN(e1i); PIN(o_z); PIN(o_cz); PIN(tw2);
    publish_kv(Int<SSMPOS == 0 ? 3 : 0>{}, []() {});
    if (MPOS == 0 && jobM) signal(cnt, 12);
    if (SSMPOS == 1) taps();
    asm volatile("" : "+v"(kx) :: "memory");
    STAMP(21);
    {
      char* kc = lds + L_KC + ch * (4 * KC_STRIDE) + (half * 128 + lane + 2) * 16;
      u32x4 kd[2];
      u32x2 kn[2];
#pragma unroll
      for (int i = 0; i < 2; ++i) { kd[i] = *(const u32x4*)(kc + i * 1024); kn[i] = *(const u32x2*)(kc + i * 1024 + 16); }
      if (lane == 63) kn[1] = kx;
#pragma unroll
      for (int i = 0; i < 2; ++i) {
        const u32 a01 = __builtin_amdgcn_alignbyte(kd[i].y, kd[i].x, 2), a12 = __builtin_amdgcn_alignbyte(kd[i].z, kd[i].y, 2);
        const u32 a23 = __builtin_amdgcn_alignbyte(kd[i].w, kd[i].z, 2), a34 = __builtin_amdgcn_alignbyte(kn[i].x, kd[i].w, 2);
        const u32 a45 = __builtin_amdgcn_alignbyte(kn[i].y, kn[i].x, 2);
        *(u32x4*)(kc + i * 1024 + KC_STRIDE) = (u32x4){a01, a12, a23, a34};
        *(u32x4*)(kc + i * 1024 + 2 * KC_STRIDE) = (u32x4){kd[i].y, kd[i].z, kd[i].w, kn[i].x};
        *(u32x4*)(kc + i * 1024 + 3 * KC_STRIDE) = (u32x4){a12, a23, a34, a45};
        if (i == 0 && half == 0 && lane == 0) {
          *(u32*)(kc - 4 + KC_STRIDE) = kd[0].x << 16;
          *(u32*)(kc - 4 + 2 * KC_STRIDE) = kd[0].x;
          *(u32x2*)(kc - 8 + 3 * KC_STRIDE) = (u32x2){kd[0].x << 16, a01};
        }
      }
    }
    STAMP(22);
    if (ch == 0) { if (arrive(cnt, 6) != 1) await<0>(cnt, 6, 2); } else { if (arrive(cnt, 7) != 1) await<0>(cnt, 7, 2); }
    STAMP(3);
    if (E1SLP < 0) drain(); else if (E1SLP) __builtin_amdgcn_s_sleep(E1SLP);
    STAMP(4);
    u32x4 x0 = ld128(rW, e1i, SC1), x1 = ld128(rW, e1i + 4096, SC1);
    u32x2 c0 = __builtin_amdgcn_raw_buffer_load_b64(rW, cki, 0, SC1), c1 = __builtin_amdgcn_raw_buffer_load_b64(rW, cki + 2048, 0, SC1);
    FENCE();
    if (MPOS == 2 && jobM) signal(cnt, 12);
    u32x2 Al[32], Ah[32];
    {
      const u32 ka = ldsb + L_KC + ch * (4 * KC_STRIDE) + ((1 + r) & 3) * KC_STRIDE + 2 * (128 * half + 8 * h + ((1 + r) & ~3));
#define RD4(i) asm volatile("ds_read_b64 %0, %8 offset:%9\n\tds_read_b64 %1, %8 offset:%9+8\n\tds_read_b64 %2, %8 offset:%9+64\n\t" \
      "ds_read_b64 %3, %8 offset:%9+72\n\tds_read_b64 %4, %8 offset:%9+128\n\tds_read_b64 %5, %8 offset:%9+136\n\t" \
      "ds_read_b64 %6, %8 offset:%9+192\n\tds_read_b64 %7, %8 offset:%9+200" \
      : "=&v"(Al[i]), "=&v"(Ah[i]), "=&v"(Al[8 + i]), "=&v"(Ah[8 + i]), "=&v"(Al[16 + i]), "=&v"(Ah[16 + i]), "=&v"(Al[24 + i]), "=&v"(Ah[24 + i]) \
      : "v"(ka), "i"(512 * i) : "memory")
      RD4(0); RD4(1); RD4(2); RD4(3); RD4(4); RD4(5); RD4(6); RD4(7);
      asm volatile("s_waitcnt lgkmcnt(0)" ::: "memory");
      PIN8(Al); PIN8(Al + 8); PIN8(Al + 16); PIN8(Al + 24); PIN8(Ah); PIN8(Ah + 8); PIN8(Ah + 16); PIN8(Ah + 24);
    }
    STAMP(5);
    {
      auto bad = [&]() __attribute__((always_inline)) {
        u32 h[4] = {fold2(x0.x, x0.y), fold2(x0.z, x0.w), fold2(x1.x, x1.y), fold2(x1.z, x1.w)};
#pragma unroll
        for (int i = 0; i < 4; ++i) h[i] += dppu<DPP_X1>(h[i]);
#pragma unroll
        for (int i = 0; i < 4; ++i) h[i] += dppu<DPP_X2>(h[i]);
#pragma unroll
        for (int i = 0; i < 4; ++i) h[i] += dppu<DPP_HM>(h[i]);
        return (((h[0] + tagv.x) ^ c0.x) | ((h[1] + tagv.y) ^ c0.y) | ((h[2] + tagv.x) ^ c1.x) | ((h[3] + tagv.y) ^ c1.y)) != 0;
      };
      bool nb = bad();
      RETRY(__any(nb)) {
        FENCE();
        if (nb) {
          x0 = ld128(rW, e1i, SC1); x1 = ld128(rW, e1i + 4096, SC1);
          c0 = __builtin_amdgcn_raw_buffer_load_b64(rW, cki, 0, SC1); c1 = __builtin_amdgcn_raw_buffer_load_b64(rW, cki + 2048, 0, SC1);
        }
        asm volatile("" : "+v"(x0), "+v"(x1), "+v"(c0), "+v"(c1));
        nb = bad();
      }
      STAMP(1);
      char* u0 = lds + L_U + ((tid >> 2) & 1) * 4608 + (30 + (tid >> 3) * 4 + (tid & 3)) * 16;
      *(u32x4*)(u0) = x0;
      *(u32x4*)(u0 + 2048) = x1;
    }
    if (arrive(cnt, 3) != 3) await<0>(cnt, 3, 4);
    STAMP(6);
    {
      f32x4 acc[8];
#pragma unroll
      for (int t = 0; t < 8; ++t) acc[t] = (f32x4){0, 0, 0, 0};
      const char* ub = lds + L_U + ch * 4608 + (30 + 2 * r + 1 - 16 * half - h) * 16;
      char* rp = lds + L_KC + w * 8192 + lane * 16;
      u32x4 B0[8], B1[8];
#pragma unroll
      for (int t = 0; t < 8; ++t) B0[t] = *(const u32x4*)(ub + 512 * t);
#pragma unroll
      for (int t = 0; t < 8; ++t) B1[t] = *(const u32x4*)(ub + 512 * t - 64);
      SCHED();
#define AF(ww, p) (u32x4){Al[8 * (ww) + (p)].x, Al[8 * (ww) + (p)].y, Ah[8 * (ww) + (p)].x, Ah[8 * (ww) + (p)].y}
#pragma unroll
      for (int bp = 0; bp < 8; ++bp) {
#pragma unroll
        for (int t = 7; t >= bp; --t) acc[t] = mfma(AF(0, t - bp), B0[bp], acc[t]);
        SCHED();
      }
#pragma unroll
      for (int bp = 0; bp < 8; ++bp) {
        B0[bp] = *(const u32x4*)(ub + 512 * bp - 128);
#pragma unroll
        for (int t = 7; t >= bp; --t) acc[t] = mfma(AF(1, t - bp), B1[bp], acc[t]);
        SCHED();
      }
#pragma unroll
      for (int bp = 0; bp < 8; ++bp) {
        B1[bp] = *(const u32x4*)(ub + 512 * bp - 192);
#pragma unroll
        for (int t = 7; t >= bp; --t) acc[t] = mfma(AF(2, t - bp), B0[bp], acc[t]);
        SCHED();
      }
#pragma unroll
      for (int t = 0; t < 8; ++t) {
#pragma unroll
        for (int bp = 0; bp <= t; ++bp) {
          acc[t] = mfma(AF(3, t - bp), B1[bp], acc[t]);
          if (t >= 2 && bp == 1) { SCHED(); *(f32x4*)(rp + (t - 1) * 1024) = acc[t - 1]; if (t == 2) *(f32x4*)(rp) = acc[0]; SCHED(); }
        }
        SCHED();
      }
      *(f32x4*)(rp + 7 * 1024) = acc[7];
    }
    STAMP(7);
    if (arrive(cnt, 14) != 7) await<0>(cnt, 14, 8);
    u32x4 zt[2];
#pragma unroll
    for (int t = 0; t < 2; ++t) {
      const char* rp = lds + L_KC + (2 * w + t) * 1024 + lane * 16;
      const char* dp = lds + L_KD + (2 * w + t) * 1024 + lane * 16;
      const f32x4 ya = *(const f32x4*)(rp) + *(const f32x4*)(rp + 8192) + *(const f32x4*)(dp);
      const f32x4 yb = *(const f32x4*)(rp + 16384) + *(const f32x4*)(rp + 24576) + *(const f32x4*)(dp + 8192);
      zt[t] = (u32x4){pk(ya[0], yb[0]), pk(ya[1], yb[1]), pk(ya[2], yb[2]), pk(ya[3], yb[3])};
      st128(zt[t], rW, o_z + 1024 * t);
    }
    FENCE();
#pragma unroll
    for (int t = 0; t < 2; ++t) {
      auto u = __builtin_amdgcn_permlane16_swap(fold2(zt[t].x, zt[t].y), fold2(zt[t].z, zt[t].w), false, false);
      __builtin_amdgcn_raw_buffer_store_b32(u[0] + u[1] + tw2, rW, o_cz + 256 * t, 0, SC1);
    }
    signal(cnt, 9);
    STAMP(15);
    u32x4 Wf[14];
    const int wvo = jobG ? (w * 448 + (lane >> 1)) * 1024 + gd0 * 2 + (lane & 1) * 16 : (w * 128 + (lane >> 1)) * (F * 2) + f0 * 2 + (lane & 1) * 16;
    if (F1POS == 0 && jobF) {
#pragma unroll
      for (int i = 0; i < 4; ++i) dma16(mkr(gwg, D * F * 2), wvo, i * 32 * F * 2, ldsb + L_W + w * 4096 + i * 1024);
      asm volatile("s_waitcnt vmcnt(4)" ::: "memory");
    } else if (jobG) {
#pragma unroll
      for (int i = 0; i < 4; ++i) dma16(mkr(gwd, F * D * 2), wvo, i * 32 * 1024, ldsb + L_W + w * 14336 + i * 1024);
      asm volatile("s_waitcnt vmcnt(4)" ::: "memory");
    } else {
      drain();
    }
    STAMP(8);
    {
      const int zo = OFF_E2 + (tid >> 1) * 8192 + j * 32 + (tid & 1) * 16, zc = OFF_CZ + (tid >> 1) * 2048 + j * 8 + (tid & 1) * 4;
      u32x4 z0 = ld128(rW, zo, SC1), z1 = ld128(rW, zo + 128 * 8192, SC1);
      u32 k0 = __builtin_amdgcn_raw_buffer_load_b32(rW, zc, 0, SC1), k1 = __builtin_amdgcn_raw_buffer_load_b32(rW, zc + 128 * 2048, 0, SC1);
      const u32 tw3 = (lane & 1) ? tagv.y : tagv.x;
      auto bad = [&]() __attribute__((always_inline)) {
        const u32 a0 = fold2(z0.x, z0.y), a1 = fold2(z0.z, z0.w), b0 = fold2(z1.x, z1.y), b1 = fold2(z1.z, z1.w);
        const u32 sa0 = a0 + dppu<DPP_X1>(a0), sa1 = a1 + dppu<DPP_X1>(a1), sb0 = b0 + dppu<DPP_X1>(b0), sb1 = b1 + dppu<DPP_X1>(b1);
        u32 d = ((((lane & 1) ? sa1 : sa0) + tw3) ^ k0) | ((((lane & 1) ? sb1 : sb0) + tw3) ^ k1);
        return d;
      };
      u32 nb = bad();
      STAMP(17);
      RETRY(__any(nb != 0)) {
        FENCE();
        nb |= dppu<DPP_X1>(nb);
        if (nb) {
          z0 = ld128(rW, zo, SC1); z1 = ld128(rW, zo + 128 * 8192, SC1);
          k0 = __builtin_amdgcn_raw_buffer_load_b32(rW, zc, 0, SC1); k1 = __builtin_amdgcn_raw_buffer_load_b32(rW, zc + 128 * 2048, 0, SC1);
        }
        asm volatile("" : "+v"(z0), "+v"(z1), "+v"(k0), "+v"(k1));
        nb = bad();
      }
      STAMP(9);
      char* zp = lds + L_Z + (tid & 1) * 4160 + (tid >> 1) * 4;
#pragma unroll
      for (int e = 0; e < 4; ++e) { *(u32*)(zp + e * 1040) = z0[e]; *(u32*)(zp + e * 1040 + 512) = z1[e]; }
    }
    bar();
    if (!(jobF || jobG)) { FLUSH(); return; }
    await<2>(cnt, 10, 4);
    if (jobG) {
#pragma unroll
      for (int i = 4; i < 8; ++i) dma16(mkr(gwd, F * D * 2), wvo, i * 32 * 1024, ldsb + L_W + w * 14336 + i * 1024);
    } else {
#pragma unroll
      for (int i = 0; i < 4; ++i) dma16(mkr(gwu, D * F * 2), wvo, i * 32 * F * 2, ldsb + L_W + 16384 + w * 4096 + i * 1024);
    }
    bar();
    if (KSLP) __builtin_amdgcn_s_sleep(KSLP);
    STAMP(11);
    const u64 tb6 = __builtin_amdgcn_s_memrealtime();
    const int ko = OFF_RK + g * 256 * RKS + (tid >> 1) * RKS + (tid & 1) * 16;
    u32x4 ka = ld128(rW, ko, SC1), kb = ld128(rW, ko + 128 * RKS, SC1);
    if (jobF) {
      asm volatile("s_waitcnt vmcnt(2)" ::: "memory");
      const char* wp = lds + L_W + w * 4096 + h * 256 + r * 8;
#pragma unroll
      for (int i = 0; i < 8; ++i) Wf[i] = trfrag(wp + (i >> 2) * 16384 + (i & 3) * 1024, 128);
      PIN8(Wf);
    }
    asm volatile("" : "+v"(ka), "+v"(kb) :: "memory");
    u32 mx = max(umax4(ka), umax4(kb));
    RETRY(!__all(mx != SENT)) {
      FENCE();
      if (mx == SENT) { ka = ld128(rW, ko, SC1); kb = ld128(rW, ko + 128 * RKS, SC1); mx = max(umax4(ka), umax4(kb)); }
    }
    if (jobG) {
#pragma unroll
      for (int i = 8; i < 14; ++i) dma16(mkr(gwd, F * D * 2), wvo, i * 32 * 1024, ldsb + L_W + w * 14336 + i * 1024);
    }
    STAMP(12);
    u32 gm = max(mx, dppu<DPP_X1>(mx));
    gm = max(gm, dppu<DPP_X2>(gm)); gm = max(gm, dppu<DPP_HM>(gm)); gm = max(gm, dppu<DPP_M>(gm));
    if (r == 0) *(u32*)(lds + L_GM + (4 * w + h) * 4) = gm;
    const u32 ky[8] = {ka.x, ka.y, ka.z, ka.w, kb.x, kb.y, kb.z, kb.w};
    u32 m2 = 0;
#pragma unroll
    for (int e = 0; e < 8; ++e) m2 = max(m2, ky[e] == mx ? 0u : ky[e]);
    PIN(m2);
    bar();
    u32 mine;
    u32 rk = rank16(lds + L_GM, lane, mine);
    const u32 T = __builtin_amdgcn_readlane(mine, __builtin_ctzll(__ballot(rk == 7)));
    if (mx >= T) {
      const u32 pos = __hip_atomic_fetch_add((u32*)(lds + L_CNT), 1u, __ATOMIC_RELAXED, __HIP_MEMORY_SCOPE_WORKGROUP);
      *(u32*)(lds + L_LIST + pos * 4) = mx;
    }
    if (__any(m2 >= T)) {
#pragma unroll
      for (int e = 0; e < 8; ++e)
        if (ky[e] >= T && ky[e] != mx) {
          const u32 pos = __hip_atomic_fetch_add((u32*)(lds + L_CNT), 1u, __ATOMIC_RELAXED, __HIP_MEMORY_SCOPE_WORKGROUP);
          *(u32*)(lds + L_LIST + pos * 4) = ky[e];
        }
    }
    bar();
    u32 cand = *(const u32*)(lds + L_LIST + r * 4);
    const u32 N = peek(cnt, 0);
    if (N > 16) {
      for (u32 i = tid; i < N; i += 256) {
        const u32 kx = *(const u32*)(lds + L_LIST + i * 4);
        u32 c = 0;
#pragma unroll 1
        for (u32 q = 0; q < N; ++q) c += *(const u32*)(lds + L_LIST + q * 4) > kx;
        if (c < 8) *(u32*)(lds + L_SEL + c * 4) = kx;
      }
      if (arrive(cnt, 15) != 3) await<0>(cnt, 15, 4);
      if (tid < 16) *(u32*)(lds + L_LIST + tid * 4) = tid < 8 ? *(const u32*)(lds + L_SEL + tid * 4) : 0u;
      if (arrive(cnt, 15) != 7) await<0>(cnt, 15, 8);
      cand = *(const u32*)(lds + L_LIST + r * 4);
    }
    STAMP(13);
    if (jobF) {
      const int ho = OFF_H2 + (2047 - (int)(cand & 0x7ffu)) * 1024 + (w * 128 + 8 * h) * 2;
      u32x4 a[4];
#pragma unroll
      for (int ks = 0; ks < 4; ++ks) a[ks] = ld128(rW, ho + ks * 64, SC1);
      FENCE();
      rk = rank16(lds + L_LIST, lane, mine);
      if (w == 0 && lane < 16 && rk < 8) *(u32*)(lds + L_RANK + rk * 4) = (lane >> 2) * 256 + (lane & 3) * 4;
      RETRY(!__all(max(max(umax4(a[0]), umax4(a[1])), max(umax4(a[2]), umax4(a[3]))) != SENT)) {
        FENCE();
#pragma unroll
        for (int ks = 0; ks < 4; ++ks) a[ks] = ld128(rW, ho + ks * 64, SC1);
        PIN(a[0]); PIN(a[1]); PIN(a[2]); PIN(a[3]);
      }
      f32x4 ag = {0, 0, 0, 0}, au = {0, 0, 0, 0};
#pragma unroll
      for (int ks = 0; ks < 4; ++ks) { ag = mfma(a[ks], Wf[ks], ag); au = mfma(a[ks], Wf[4 + ks], au); }
      *(f32x4*)(lds + L_KC + w * 2048 + lane * 16) = ag;
      *(f32x4*)(lds + L_KC + w * 2048 + 1024 + lane * 16) = au;
      bar();
      STAMP(14);
      if (w < 2) {
        const char* p = lds + L_KC + *(const u32*)(lds + L_RANK + (lane >> 3) * 4) + (8 * w + (lane & 7)) * 16;
        float gv = 0, uv = 0;
#pragma unroll
        for (int i = 0; i < 4; ++i) { gv += *(const float*)(p + i * 2048); uv += *(const float*)(p + i * 2048 + 1024); }
        gv = rb(gv);
        const float v = rb(gv * __builtin_amdgcn_rcpf(1.f + __expf(-gv))) * rb(uv);
        const float vn = dppf<DPP_X1>(v);
        __builtin_amdgcn_raw_buffer_store_b32(nos(pk(v, vn)), rW, (lane & 1) ? OOB : OFF_GU + f0 * 16 + w * 128 + lane * 2, 0, SC1);
        STAMP(10);
      }
    } else {
      rk = rank16(lds + L_LIST, lane, mine);
      if (w == 0 && lane < 16 && rk < 8) *(u32*)(lds + L_SEL + rk * 4) = mine;
      bar();
      drain();
      {
        const char* wp = lds + L_W + w * 14336 + h * 256 + r * 8;
#pragma unroll
        for (int i = 0; i < 14; ++i) Wf[i] = trfrag(wp + i * 1024, 128);
        PIN8(Wf); PIN8(Wf + 6);
      }
      while (__builtin_amdgcn_s_memrealtime() < tb6 + GDELAY) __builtin_amdgcn_s_sleep(1);
      STAMP(14);
      const int go = OFF_GU + (w * 56 + 4 * (r >> 3) + h) * 128 + (r & 7) * 16;
      u32x4 a[7];
#pragma unroll 1
      for (int spins = 0; spins < SPIN_CAP; ++spins) {
        FENCE();
        u32 mm = 0;
#pragma unroll
        for (int i = 0; i < 7; ++i) a[i] = ld128(rW, go + i * 1024, SC1);
#pragma unroll
        for (int i = 0; i < 7; ++i) mm = max(mm, umax4(a[i]));
        if (__all(mm != SENT)) break;
      }
      STAMP(10);
      f32x4 a0 = {0, 0, 0, 0}, a1 = {0, 0, 0, 0};
#pragma unroll
      for (int i = 0; i < 7; ++i) { a0 = mfma(a[i], Wf[2 * i], a0); a1 = mfma(a[i], Wf[2 * i + 1], a1); }
      *(f32x4*)(lds + L_KC + w * 1024 + lane * 16) = lane < 32 ? a0 : a1;
      bar();
    }
    FLUSH();
  } else {
    const int a = w - 4, at = tid - 256, hl = lane & 31, row = 8 * j + 2 * a + (lane >> 5);
    __builtin_amdgcn_s_setprio(1);
    u32 cw = __builtin_amdgcn_raw_buffer_load_b32(mkr(gc, D * 2), jobM ? at * 4 : OOB, 0, 0);
    u32 dpair = __builtin_amdgcn_raw_buffer_load_b32(mkr(gssmD, D * 2), j * 4, 0, 0);
    FENCE();
    if (PF0SLP) __builtin_amdgcn_s_sleep(PF0SLP);
    u32x2 wrw = __builtin_amdgcn_raw_buffer_load_b64(mkr(gwr, D * 2), (at & 127) * 8, 0, 0);
    const u16* pP0 = jobG ? gwd : gwmod;
    PINS(pP0);
    const rsrc_t rP0 = mkr(pP0, D * 6 * D * 2);
    const int pf_g = m * 57344 + at * 128, pf_m = (m * 16 + (at >> 3)) * 6144 + g * 1024 + (at & 7) * 128;
    const int pf_n = jobG ? 192 : (g < 6 ? 128 : 0);
    int o_pf0 = at < pf_n ? (jobG ? pf_g : pf_m) : OOB;
    u32 pf0;
    if (PFPOS == 0) pf0 = __builtin_amdgcn_raw_buffer_load_b32(rP0, o_pf0, 0, NT);
    FENCE();
    EPOCH();
    MAKE_TAG();
    int o_own = j * 8192 + at * 16;
    int o_m = (a * 128 + (lane >> 2)) * 6144 + mcol * 2 + (lane & 3) * 16;
    int o_w = L_W + a * 8192 + ((lane >> 4) * 8 + ((lane & 15) >> 2)) * 64 + (lane & 3) * 8;
    asm volatile("" : "+v"(o_own), "+v"(o_m), "+v"(o_w), "+v"(s4), "+v"(o_pf0));
    publish_kv(Int<PFPOS == 0 ? 4 : 3>{}, [&]() __attribute__((always_inline)) {
      if (PFPOS == 1) { pf0 = __builtin_amdgcn_raw_buffer_load_b32(rP0, o_pf0, 0, NT); FENCE(); }
    });
    asm volatile("" : "+v"(dpair), "+v"(cw), "+v"(wrw) :: "memory");
    u32x4 av[4];
    auto mod_request = [&]() __attribute__((always_inline)) {
      const rsrc_t rM = mkr(gwmod, D * 6 * D * 2);
#pragma unroll
      for (int i = 0; i < 8; ++i) dma16(rM, o_m, i * 16 * 6144, ldsb + L_W + a * 8192 + i * 1024);
    };
    auto mod_compute = [&]() __attribute__((always_inline)) {
      STAMP(21);
      u32x4 bw[8];
#pragma unroll
      for (int i = 0; i < 8; ++i) bw[i] = trfrag(lds + o_w + (i >> 1) * 2048 + (i & 1) * 32, 256);
      PIN8(bw);
      f32x4 d0 = {0, 0, 0, 0}, d1 = {0, 0, 0, 0};
#pragma unroll
      for (int ks = 0; ks < 4; ++ks) { d0 = mfma(av[ks], bw[2 * ks], d0); d1 = mfma(av[ks], bw[2 * ks + 1], d1); }
      if (lane < 16) { *(float*)(lds + L_MP + a * 128 + lane * 4) = d0[0]; *(float*)(lds + L_MP + a * 128 + 64 + lane * 4) = d1[0]; }
      const u32 nth = arrive(cnt, 2);
      {
        f32x4 pp[4];
#pragma unroll
        for (int i = 0; i < 4; ++i) pp[i] = *(const f32x4*)(lds + L_MP + i * 128 + (lane & 7) * 16);
        float sm[4];
#pragma unroll
        for (int e = 0; e < 4; ++e) sm[e] = add1(add1(pp[0][e], pp[1][e]), add1(pp[2][e], pp[3][e]));
        const u32 p0 = pk(sm[0], sm[1]), p1 = pk(sm[2], sm[3]);
        st128((u32x4){p0, p1, p0 ^ tagv.x, p1 ^ tagv.y}, rW, nth == 3 ? OFF_MOD + (lane >> 3) * 12288 + mcol * 4 + (lane & 7) * 16 : OOB);
        FENCE();
      }
      STAMP(3);
    };
    if (jobM) {
      if (MPOS == 0) { await<1>(cnt, 12, 4); mod_request(); }
      f32x2 cv = up2(cw);
      cv.x = mul1(cv.x, __builtin_amdgcn_rcpf(add1(1.f, __builtin_amdgcn_exp2f(mul1(cv.x, -1.44269504088896f)))));
      cv.y = mul1(cv.y, __builtin_amdgcn_rcpf(add1(1.f, __builtin_amdgcn_exp2f(mul1(cv.y, -1.44269504088896f)))));
      *(u32*)(lds + L_SC + at * 4) = pk2(cv);
#pragma unroll
      for (int ks = 0; ks < 4; ++ks) av[ks] = *(const u32x4*)(lds + L_SC + a * 256 + ks * 64 + (lane >> 4) * 16);
      PIN(av[0]); PIN(av[1]); PIN(av[2]); PIN(av[3]);
      if (MPOS == 2) { await<1>(cnt, 12, 4); mod_request(); }
      if (MPOS == 0 || MPOS == 2) { drain(); mod_compute(); }
    }
    int o_sm = OOB, o_q = row * 1024 + hl * 16;
    if (at < 16) o_sm = OFF_RK + (at >> 1) * 256 * RKS + j * RKS + (at & 1) * 16;
    if (jobF && at >= 16 && at < 32) o_sm = OFF_GU + f0 * 16 + (at - 16) * 16;
    int o_pf = jobG ? m * 57344 + 24576 + at * 128 : (a < 2 ? (m * 16 + (lane >> 2)) * (F * 2) + g * 512 + (lane & 3) * 128 : OOB);
    const int un = 128 * a + 4 * (lane & 15) + (lane >> 4);
    int o_u = L_U + (30 + (un >> 1)) * 16 + ((un & 1) ? 0 : 8);
    int o_mod = OFF_MOD + g * 12288 + (a >> 1) * 6144 + (at & 127) * 16;
    asm volatile("" : "+v"(o_sm), "+v"(o_q), "+v"(o_pf), "+v"(o_u), "+v"(o_mod));
    await<1>(cnt, 3, 4);
    STAMP(6);
    PIN(pf0);
    if (MPOS == 1 && jobM) { mod_request(); if (arrive(cnt, 12) != 3) await<1>(cnt, 12, 4); }
    __builtin_amdgcn_raw_buffer_store_b128(s4, rW, o_own, OFF_H2, SC1);
    __builtin_amdgcn_raw_buffer_store_b128(s4, rW, o_own, OFF_H2 + 4096, SC1);
    st128(s4, rW, o_sm);
    FENCE();
    u32x4 q0 = ld128(mkr(gq, S * D * 2), o_q, NT), q1 = ld128(mkr(gq, S * D * 2), o_q + 512, NT);
    FENCE();
    u32 pf1 = __builtin_amdgcn_raw_buffer_load_b32(mkr(jobG ? gwd : (a == 0 ? gwg : gwu), D * F * 2), o_pf, 0, NT);
    FENCE();
    {
      const f32x2 dv = up2(dpair);
#pragma unroll
      for (int t = 0; t < 2; ++t) {
        const u32x2 ua = *(const u32x2*)(lds + o_u + 512 * t), ub = *(const u32x2*)(lds + o_u + 512 * t + 4608);
        char* dp = lds + L_KD + (2 * a + t) * 1024 + lane * 16;
        *(f32x4*)(dp) = (f32x4){mul0(hi2f(ua.y), dv.x), mul0(lo2f(ua.y), dv.x), mul0(hi2f(ua.x), dv.x), mul0(lo2f(ua.x), dv.x)};
        *(f32x4*)(dp + 8192) = (f32x4){mul0(hi2f(ub.y), dv.y), mul0(lo2f(ub.y), dv.y), mul0(hi2f(ub.x), dv.y), mul0(lo2f(ub.x), dv.y)};
      }
    }
    if (MPOS == 1 && jobM) {
      asm volatile("s_waitcnt vmcnt(6)" ::: "memory");
      mod_compute();
      asm volatile("s_waitcnt vmcnt(4)" ::: "memory");
    } else {
      asm volatile("s_waitcnt vmcnt(3)" ::: "memory");
    }
    signal(cnt, 14);
    STAMP(5);
    asm volatile("" : "+v"(q0), "+v"(q1) :: "memory");
    STAMP(7);
    __builtin_amdgcn_s_setprio(0);
    f32x2 x[8], gah[8];
    {
      unpack16(q0, q1, x);
      float s = 0;
#pragma unroll
      for (int e = 0; e < 8; ++e) s = add0(s, add0(x[e].x, x[e].y));
      const float mean = half_sum(s) * (1.f / 512.f);
      float qq = 0;
#pragma unroll
      for (int e = 0; e < 8; ++e) { const float d0 = add0(x[e].x, -mean), d1 = add0(x[e].y, -mean); qq = fma0(d0, d0, qq); qq = fma0(d1, d1, qq); }
      const float var = rb(half_sum(qq) * (1.f / 512.f));
      float isd = __builtin_amdgcn_rcpf(rb(__builtin_amdgcn_sqrtf(rb(var + 1e-5f))));
      float mi = -rb(mean) * isd;
      PIN(isd); PIN(mi);
#pragma unroll
      for (int e = 0; e < 8; ++e) gah[e] = (f32x2){fma0(x[e].x, isd, mi), fma0(x[e].y, isd, mi)};
      PIN8(x); PIN8(gah);
    }
    STAMP(8);
    asm volatile("" : "+v"(pf1) :: "memory");
    if (F1POS == 1 && jobF) {
#pragma unroll
      for (int i = 0; i < 4; ++i) dma16(mkr(gwg, D * F * 2), (a * 128 + (lane >> 1)) * (F * 2) + f0 * 2 + (lane & 1) * 16, i * 32 * F * 2, ldsb + L_W + a * 4096 + i * 1024);
    }
    STAMP(4);
    if (MRD == 0) await<1>(cnt, 9, 4);
    if (MODSLP) __builtin_amdgcn_s_sleep(MODSLP);
    {
      u32x4 mu[3];
#pragma unroll
      for (int i = 0; i < 3; ++i) mu[i] = ld128(rW, o_mod + i * 2048, SC1);
      auto modbad = [&]() __attribute__((always_inline)) {
        u32 d = 0;
#pragma unroll
        for (int i = 0; i < 3; ++i) d |= (mu[i].x ^ mu[i].z ^ tagv.x) | (mu[i].y ^ mu[i].w ^ tagv.y);
        return d != 0;
      };
      bool nb = modbad();
      RETRY(__any(nb)) {
        FENCE();
        __builtin_amdgcn_s_sleep(3);
        if (nb) {
#pragma unroll
          for (int i = 0; i < 3; ++i) mu[i] = ld128(rW, o_mod + i * 2048, SC1);
        }
        asm volatile("" : "+v"(mu[0]), "+v"(mu[1]), "+v"(mu[2]));
        nb = modbad();
      }
      __builtin_amdgcn_s_setprio(1);
      STAMP(17);
      const f32x2 s0 = up2(mu[0].x), s1 = up2(mu[0].y), c0 = rb2(up2(mu[1].x) + 1.f), c1 = rb2(up2(mu[1].y) + 1.f), g0 = up2(mu[2].x), g1 = up2(mu[2].y);
      char* mo = lds + L_MOD + (at & 1) * 1024 + ((at & 127) >> 1) * 16;
      auto put = [&](int v, f32x2 lo, f32x2 hi) __attribute__((always_inline)) { *(f32x4*)(mo + v * 2048) = (f32x4){lo.x, lo.y, hi.x, hi.y}; };
      if (a < 2) {
        put(0, g0 * c0, g1 * c1);
        put(1, g0 * s0, g1 * s1);
      } else {
        const f32x2 w0 = up2(wrw.x), w1 = up2(wrw.y), r0 = c0 * w0, r1 = c1 * w1, sa_ = r0 + r1, sc_ = s0 * w0 + s1 * w1;
        put(2, r0, r1); put(3, c0, c1); put(4, s0, s1); put(5, g0, g1);
        const float pa = half_sum(sa_.x + sa_.y), pc2 = half_sum(sc_.x + sc_.y);
        if (hl == 0) *(f32x2*)(lds + L_MOD + 12288 + ((a & 1) * 2 + (lane >> 5)) * 8) = (f32x2){pa, pc2};
      }
      STAMP(9);
      if (arrive(cnt, 13) != 3) await<1>(cnt, 13, 4);
    }
    f32x2 ra[8], cb1[8], sb[8], gb[8];
    float suma, sumc;
    {
      const char* mp = lds + L_MOD + hl * 16;
      auto vec16 = [&](int v, f32x2* o) __attribute__((always_inline)) {
#pragma unroll
        for (int i = 0; i < 4; ++i) {
          const f32x4 t = *(const f32x4*)(mp + v * 2048 + (i >> 1) * 512 + (i & 1) * 1024);
          o[2 * i] = (f32x2){t[0], t[1]}; o[2 * i + 1] = (f32x2){t[2], t[3]};
        }
      };
      f32x2 a1[8], a0[8];
      vec16(0, a1); vec16(1, a0); vec16(2, ra); vec16(3, cb1); vec16(4, sb); vec16(5, gb);
      const f32x4 pa = *(const f32x4*)(lds + L_MOD + 12288), pc2 = *(const f32x4*)(lds + L_MOD + 12288 + 16);
#pragma unroll
      for (int e = 0; e < 8; ++e) gah[e] = gah[e] * a1[e] + a0[e];
      suma = (pa[0] + pa[2]) + (pc2[0] + pc2[2]);
      sumc = (pa[1] + pa[3]) + (pc2[1] + pc2[3]);
      PIN8(gah); PIN8(ra); PIN8(cb1); PIN8(sb); PIN8(gb); PIN(suma); PIN(sumc);
    }
    int o_key = hl < 8 ? OFF_RK + hl * 256 * RKS + j * RKS + (row & 7) * 4 : OOB;
    PIN(o_key);
    STAMP(10);
    bar();
    STAMP(11);
    {
      f32x2 z[8];
      const char* zr = lds + L_Z + (2 * a + (lane >> 5)) * 1040 + hl * 16;
      unpack16(*(const u32x4*)(zr), *(const u32x4*)(zr + 512), z);
      f32x2 s1 = {0, 0}, s2 = {0, 0}, s3 = {0, 0};
#pragma unroll
      for (int e = 0; e < 8; ++e) { x[e] += gah[e] * z[e]; s1 += x[e]; s2 += x[e] * x[e]; s3 += x[e] * ra[e]; }
      const float t1 = half_sum(s1.x + s1.y), t2 = half_sum(s2.x + s2.y), t3 = half_sum(s3.x + s3.y);
      const float mean = t1 * (1.f / 512.f);
      const f32x2 mvb = rb2((f32x2){mean, fmaxf(t2 * (1.f / 512.f) - mean * mean, 0.f)});
      const float mb = mvb.x, isd = __builtin_amdgcn_rcpf(rb(__builtin_amdgcn_sqrtf(rb(mvb.y + 1e-5f))));
      const float rr = (t3 - mb * suma) * isd + sumc;
      u32 kb = pk(0.f, rr);
      kb ^= ((int)kb < 0) ? 0xffff0000u : 0x80000000u;
      __builtin_amdgcn_raw_buffer_store_b32(kb | 0x8000u | (u32)(2047 - row), rW, o_key, 0, SC1);
      signal(cnt, 10);
      STAMP(12);
      u32 hw[8], ow[8];
#pragma unroll
      for (int e = 0; e < 8; ++e) {
        const f32x2 h2 = (x[e] - mb) * isd * cb1[e] + sb[e];
        hw[e] = nos(pk2(h2));
        ow[e] = pk2(x[e] + gb[e] * h2);
      }
      PIN8(hw); PIN8(ow);
      drain();
      bar();
      STAMP(13);
      st128((u32x4){hw[0], hw[1], hw[2], hw[3]}, rW, OFF_H2 + row * 1024 + hl * 16);
      st128((u32x4){hw[4], hw[5], hw[6], hw[7]}, rW, OFF_H2 + row * 1024 + 512 + hl * 16);
      st128((u32x4){ow[0], ow[1], ow[2], ow[3]}, rO, row * 1024 + hl * 16);
      st128((u32x4){ow[4], ow[5], ow[6], ow[7]}, rO, row * 1024 + 512 + hl * 16);
    }
    if (b == 0 && a == 0) __builtin_amdgcn_raw_buffer_store_b64(tagv, rW, lane == 0 ? OFF_EP : OOB, 0, SC1);
    if (jobF || jobG) { bar(); bar(); }
    drain();
    STAMP(14);
    if (arrive(cnt, 5) == 3) __builtin_amdgcn_raw_buffer_store_b64(tagv, rW, lane == 0 ? OFF_FL3 + j * 128 : OOB, 0, SC1);
    if (jobG && a < 2) {
      __builtin_amdgcn_s_setprio(0);
      bar();
      const int r = lane & 15, tok = at >> 4;
      const u32 key = *(const u32*)(lds + L_SEL + tok * 4);
      const int orow = 2047 - (int)(key & 0x7ffu);
#pragma unroll 1
      for (int spins = 0; spins < SPIN_CAP; ++spins) {
        FENCE();
        const u32x2 f = __builtin_amdgcn_raw_buffer_load_b64(rW, OFF_FL3 + (orow >> 3) * 128, 0, SC1);
        if (__all(f.x == tagv.x && f.y == tagv.y)) break;
        __builtin_amdgcn_s_sleep(4);
      }
      FENCE();
      float oold = lo2f(__builtin_amdgcn_raw_buffer_load_b16(rO, orow * 1024 + (gd0 + r) * 2, 0, SC1));
      u32 rbits = key >> 16;
      rbits = (rbits & 0x8000u) ? (rbits & 0x7fffu) : (rbits ^ 0xffffu);
      float gy = rb(__builtin_amdgcn_rcpf(1.f + __expf(-lo2f(rbits)))) * *(const float*)(lds + L_MOD + 10240 + ((r >> 2) & 1) * 1024 + ((gd0 + r) >> 3) * 16 + (r & 3) * 4);
      int oo = (r & 1) ? OOB : orow * 1024 + (gd0 + r) * 2, po = L_KC + ((tok >> 2) * 16 + r) * 16 + (tok & 3) * 4;
      PIN(gy); PIN(oold); PIN(oo); PIN(po);
      STAMP(15);
      bar();
      float y = 0;
#pragma unroll
      for (int i = 0; i < 8; ++i) y += *(const float*)(lds + po + i * 512);
      const float o = oold + gy * rb(y);
      const float on = dppf<DPP_X1>(o);
      __builtin_amdgcn_raw_buffer_store_b32(pk(o, on), rO, oo, 0, SC1);
      {
        const u64 tend = __builtin_amdgcn_s_memrealtime();
        u32 mn = 0xffffffffu, mx = 0, mn2 = 0xffffffffu, mx2 = 0;
        for (int i = 0; i < 4; ++i) { const u32x4 v = ld128(rW, OFF_ST + (lane + 64 * i) * 16, SC1); const u32 d = (u32)tend - v.x, e = (u32)tend - v.z; mn = min(mn, d); mx = max(mx, d); mn2 = min(mn2, e); mx2 = max(mx2, e); }
        for (int o = 32; o; o >>= 1) { mn = min(mn, (u32)__shfl_xor((int)mn, o)); mx = max(mx, (u32)__shfl_xor((int)mx, o)); mn2 = min(mn2, (u32)__shfl_xor((int)mn2, o)); mx2 = max(mx2, (u32)__shfl_xor((int)mx2, o)); }
        u32 amt = PK == 0 ? 0u : (PK == 1 ? mx - mn : (PK == 2 ? mx : (PK == 3 ? mx - mn2 : mx - mx2)));
        amt = __builtin_amdgcn_readfirstlane(min(amt, 5000u));
        const u64 until = __builtin_amdgcn_s_memrealtime() + 4 * (u64)amt;
        while (__builtin_amdgcn_s_memrealtime() < until) __builtin_amdgcn_s_sleep(2);
      }
    }
    FLUSH();
  }
}

extern "C" void kernel_launch(void* const* d_in, const int* in_sizes, int n_in, void* d_out, int out_size, void* d_ws,
                              size_t ws_size, hipStream_t stream) {
  fused_kernel<<<256, 512, 0, stream>>>((char*)d_ws + (WSEND ? ((ws_size - WS_BYTES) & ~(size_t)4095) : 0), desc_hi(WS_BYTES), (const u16*)d_in[1], desc_hi(S * D * 2), (const u16*)d_in[2],
                                        desc_hi(S * D * 2), (const u16*)d_in[3], desc_hi(D * 8), (const u16*)d_in[4], (const u16*)d_in[11],
                                        (const u16*)d_in[12], (const u16*)d_in[0], (const u16*)d_in[5], (const u16*)d_in[7],
                                        (const u16*)d_in[8], (const u16*)d_in[9], (const u16*)d_in[10], (u16*)d_out);
}
```

```cpp
#define PST 20
#define PWR 12
#define PW 4
#define PK 0
#include <hip/hip_runtime.h>
#include <stdint.h>

typedef unsigned short u16;
typedef unsigned int u32;
typedef unsigned long long u64;
typedef short bf16x8 __attribute__((ext_vector_type(8)));
typedef short s16x4 __attribute__((ext_vector_type(4)));
typedef float f32x4 __attribute__((ext_vector_type(4)));
typedef float f32x2 __attribute__((ext_vector_type(2)));
typedef __bf16 bf16x2 __attribute__((ext_vector_type(2)));
typedef u32 u32x4 __attribute__((ext_vector_type(4)));
typedef u32 u32x2 __attribute__((ext_vector_type(2)));
typedef __amdgpu_buffer_rsrc_t rsrc_t;
#define LAS __attribute__((address_space(3)))

constexpr int S = 2048, D = 512, F = 1792;
constexpr u32 SENT = 0xFFFFFFFFu;
constexpr int SC1 = 16, NT = 2;
constexpr int SPIN_CAP = 1 << 14;
#define RKS 32
#define JOBPOS 0
#define FW0 4
#define CHKSLP 4
#define ZSLP 0
#define MPOS 0
#define CODE_BYTES 23000
#define ZPACE 8
#define KPACE 6
#define CWARM 1
#define PFAUX 0
#define WNT 1
#define E1SLP 0
#define MRD 1
#define PFPOS 0
#define F1POS 0
#define WSEND 0
#define KSLP 0
#define MODSLP 0
#define F2S 8
#define SSMPOS 0
#define F1S 8
#define PF0SLP 0
#define GDELAY 200

constexpr int OFF_E1 = 0;
constexpr int OFF_E2 = 2 << 20;
constexpr int OFF_H2 = 4 << 20;
constexpr int OFF_SM = 6 << 20;
constexpr int OFF_MOD = OFF_SM + 524288;
constexpr int OFF_RK = 7 << 20;
constexpr int OFF_GU = OFF_SM + 65536;
constexpr int OFF_CK = OFF_SM + 262144;
constexpr int OFF_CZ = (7 << 20) + 524288;
constexpr int OFF_EP = OFF_SM + 131072;
constexpr int OFF_FL3 = OFF_SM + 196608;
constexpr int OFF_ST = 8 << 20;
constexpr int WS_BYTES = 16 << 20;
constexpr int OOB = 0x7ffffff0;

constexpr int KC_STRIDE = 4160;
constexpr int L_KC = 0;
constexpr int L_U = 33280;
constexpr int L_Z = L_U + 9216;
constexpr int L_W = L_Z + 8320;
constexpr int L_KV = L_W + 57344;
constexpr int L_KD = L_KV;
constexpr int L_SC = L_KV + 18432;
constexpr int L_MOD = L_SC + 1024;
constexpr int L_MP = L_MOD + 12288 + 32;
constexpr int L_GM = L_MP + 512;
constexpr int L_CNT = L_GM + 64;
constexpr int L_RANK = L_CNT + 64;
constexpr int L_SEL = L_RANK + 64;
constexpr int L_LIST = L_SEL + 32;
constexpr int L_END = L_LIST + 4096;

#define STAMP(i) do { if ((i) == PST) tprobe = __builtin_amdgcn_s_memrealtime(); \
  if ((i) == PWR && w == PW) __builtin_amdgcn_raw_buffer_store_b64((u32x2){(u32)tstart, (u32)tprobe}, mkr(wsp, WS_BYTES), lane == 0 ? OFF_ST + b * 8 : OOB, 0, SC1); } while (0)
#define FLUSH() do { } while (0)

template <int N> struct Int { static constexpr int value = N; };
#define PIN(x) asm volatile("" : "+v"(x))
#define PINS(x) asm volatile("" : "+s"(x))
#define PIN8(a) asm volatile("" : "+v"((a)[0]), "+v"((a)[1]), "+v"((a)[2]), "+v"((a)[3]), "+v"((a)[4]), "+v"((a)[5]), "+v"((a)[6]), "+v"((a)[7]))
#define FENCE() asm volatile("" ::: "memory")
#define RETRY(bad) if (__builtin_expect(bad, 0)) _Pragma("unroll 1") for (int spins = 0; spins < SPIN_CAP && (spins == 0 || (bad)); ++spins)
#define SCHED() __builtin_amdgcn_sched_barrier(0)
__device__ __forceinline__ float lo2f(u32 w) { return __uint_as_float(w << 16); }
__device__ __forceinline__ float hi2f(u32 w) { return __uint_as_float(w & 0xffff0000u); }
__device__ __forceinline__ f32x2 up2(u32 w) { return (f32x2){lo2f(w), hi2f(w)}; }
__device__ __forceinline__ u32 pk2(f32x2 v) { return __builtin_bit_cast(u32, __builtin_convertvector(v, bf16x2)); }
__device__ __forceinline__ u32 pk(float lo, float hi) { return pk2((f32x2){lo, hi}); }
__device__ __forceinline__ u32 nos(u32 x) { return min(x, SENT - 1u); }
__device__ __forceinline__ float rb(float x) { return (float)(__bf16)x; }
__device__ __forceinline__ f32x2 rb2(f32x2 v) { return up2(pk2(v)); }
__device__ __forceinline__ float mul1(float a, float b) { float r; asm("s_nop 0\n\tv_mul_f32 %0, %1, %2" : "=v"(r) : "v"(a), "v"(b)); return r; }
__device__ __forceinline__ float add1(float a, float b) { float r; asm("s_nop 0\n\tv_add_f32 %0, %1, %2" : "=v"(r) : "v"(a), "v"(b)); return r; }
__device__ __forceinline__ float mul0(float a, float b) { float r; asm("v_mul_f32 %0, %1, %2" : "=v"(r) : "v"(a), "v"(b)); return r; }
__device__ __forceinline__ float add0(float a, float b) { float r; asm("v_add_f32 %0, %1, %2" : "=v"(r) : "v"(a), "v"(b)); return r; }
__device__ __forceinline__ float fma0(float a, float b, float c) { float r; asm("v_fma_f32 %0, %1, %2, %3" : "=v"(r) : "v"(a), "v"(b), "v"(c)); return r; }
__device__ __forceinline__ u32 fold2(u32 a, u32 b) { return a + __builtin_amdgcn_alignbit(b, b, 21); }
__device__ __forceinline__ u32 umax4(u32x4 x) { return max(max(x.x, x.y), max(x.z, x.w)); }
__device__ __forceinline__ void bar() { asm volatile("s_waitcnt lgkmcnt(0)\n\ts_barrier" ::: "memory"); }
__device__ __forceinline__ void drain() { asm volatile("s_waitcnt vmcnt(0)" ::: "memory"); }
__device__ __forceinline__ rsrc_t mkr(const void* p, int bytes) { return __builtin_amdgcn_make_buffer_rsrc((void*)p, 0, bytes, 0x00020000); }
__device__ __forceinline__ u32x4 ld128(rsrc_t r, int off, int aux) {
  return aux == SC1 ? __builtin_amdgcn_raw_buffer_load_b128(r, off, 0, SC1)
                    : (aux == NT ? __builtin_amdgcn_raw_buffer_load_b128(r, off, 0, NT) : __builtin_amdgcn_raw_buffer_load_b128(r, off, 0, 0));
}
__device__ __forceinline__ void st128(u32x4 v, rsrc_t r, int off) { __builtin_amdgcn_raw_buffer_store_b128(v, r, off, 0, SC1); }
__device__ __forceinline__ f32x4 mfma(u32x4 a, u32x4 b, f32x4 c) {
  return __builtin_amdgcn_mfma_f32_16x16x32_bf16(__builtin_bit_cast(bf16x8, a), __builtin_bit_cast(bf16x8, b), c, 0, 0, 0);
}
__device__ __forceinline__ void dma16(rsrc_t r, int voff, int soff, u32 ldsaddr) {
  asm volatile("s_mov_b32 m0, %2\n\ts_nop 0\n\tbuffer_load_dwordx4 %0, %1, %3 offen nt lds" :: "v"(voff), "s"(r), "s"(ldsaddr), "s"(soff) : "memory");
}
__device__ __forceinline__ void dmaw(rsrc_t r, int voff, int soff, u32 ldsaddr) {
  if (WNT) dma16(r, voff, soff, ldsaddr);
  else asm volatile("s_mov_b32 m0, %2\n\ts_nop 0\n\tbuffer_load_dwordx4 %0, %1, %3 offen lds" :: "v"(voff), "s"(r), "s"(ldsaddr), "s"(soff) : "memory");
}
__device__ __forceinline__ u32x2 trread(const char* p) {
  return __builtin_bit_cast(u32x2, __builtin_amdgcn_ds_read_tr16_b64_v4i16((LAS s16x4*)(LAS char*)p));
}
__device__ __forceinline__ float dot2(u32 a, u32 b, float c) {
  return __builtin_amdgcn_fdot2_f32_bf16(__builtin_bit_cast(bf16x2, a), __builtin_bit_cast(bf16x2, b), c, false);
}
__device__ __forceinline__ void dma16_sc1(rsrc_t r, int voff, int soff, u32 ldsaddr) {
  asm volatile("s_mov_b32 m0, %2\n\ts_nop 0\n\tbuffer_load_dwordx4 %0, %1, %3 offen sc1 lds" :: "v"(voff), "s"(r), "s"(ldsaddr), "s"(soff) : "memory");
}
__device__ __forceinline__ u32x4 trfrag(const char* p, int q4) {
  const u32x2 a = __builtin_bit_cast(u32x2, __builtin_amdgcn_ds_read_tr16_b64_v4i16((LAS s16x4*)(LAS char*)p));
  const u32x2 c = __builtin_bit_cast(u32x2, __builtin_amdgcn_ds_read_tr16_b64_v4i16((LAS s16x4*)(LAS char*)(p + q4)));
  return (u32x4){a.x, a.y, c.x, c.y};
}
template <int CTRL> __device__ __forceinline__ u32 dppu(u32 x) { return (u32)__builtin_amdgcn_mov_dpp((int)x, CTRL, 0xf, 0xf, true); }
template <int CTRL> __device__ __forceinline__ float dppf(float x) { return __uint_as_float(dppu<CTRL>(__float_as_uint(x))); }
constexpr int DPP_X1 = 0xB1, DPP_X2 = 0x4E, DPP_HM = 0x141, DPP_M = 0x140;
__device__ __forceinline__ float half_sum(float v) {
  v += dppf<DPP_X1>(v); v += dppf<DPP_X2>(v); v += dppf<DPP_HM>(v); v += dppf<DPP_M>(v);
  auto s = __builtin_amdgcn_permlane16_swap(__float_as_uint(v), __float_as_uint(v), false, false);
  return __uint_as_float(s[0]) + __uint_as_float(s[1]);
}
__device__ __forceinline__ void unpack16(u32x4 a, u32x4 c, f32x2* o) {
#pragma unroll
  for (int e = 0; e < 4; ++e) { o[e] = up2(a[e]); o[4 + e] = up2(c[e]); }
}
__device__ __forceinline__ u32 rank16(const char* lds, int lane, u32& mine) {
  mine = *(const u32*)(lds + (lane & 15) * 4);
  const u32x4 o = *(const u32x4*)(lds + (lane >> 4) * 16);
  u32 c = (o.x > mine) + (o.y > mine) + (o.z > mine) + (o.w > mine);
  auto s = __builtin_amdgcn_permlane16_swap(c, c, false, false);
  c = s[0] + s[1];
  auto t = __builtin_amdgcn_permlane32_swap(c, c, false, false);
  return t[0] + t[1];
}
__device__ __forceinline__ u32 arrive(u32 cnt, int idx) {
  u32 old, tmp;
  asm volatile("s_waitcnt lgkmcnt(0)\n\ts_mov_b64 exec, 1\n\tv_mov_b32 %1, 1\n\tds_add_rtn_u32 %1, %2, %1 offset:%3\n\ts_mov_b64 exec, -1\n\t"
               "s_waitcnt lgkmcnt(0)\n\tv_readfirstlane_b32 %0, %1" : "=s"(old), "=&v"(tmp) : "v"(cnt), "i"(idx * 4) : "memory");
  return old;
}
__device__ __forceinline__ void signal(u32 cnt, int idx) {
  u32 tmp;
  asm volatile("s_waitcnt lgkmcnt(0)\n\ts_mov_b64 exec, 1\n\tv_mov_b32 %0, 1\n\tds_add_u32 %1, %0 offset:%2\n\ts_mov_b64 exec, -1"
               : "=&v"(tmp) : "v"(cnt), "i"(idx * 4) : "memory");
}
__device__ __forceinline__ u32 peek(u32 cnt, int idx) {
  u32 v, tmp;
  asm volatile("ds_read_b32 %1, %2 offset:%3\n\ts_waitcnt lgkmcnt(0)\n\tv_readfirstlane_b32 %0, %1" : "=s"(v), "=&v"(tmp) : "v"(cnt), "i"(idx * 4) : "memory");
  return v;
}
template <int SLEEP> __device__ __forceinline__ void await(u32 cnt, int idx, u32 n) {
#pragma unroll 1
  for (int spins = 0; peek(cnt, idx) < n && spins < (SPIN_CAP << 6); ++spins) if (SLEEP) __builtin_amdgcn_s_sleep(SLEEP);
}

typedef u32 u32x16 __attribute__((ext_vector_type(16)));
typedef u64 u64x8 __attribute__((ext_vector_type(8)));
typedef u64 u64x2 __attribute__((ext_vector_type(2)));
constexpr u64 desc_hi(u32 bytes) { return (u64)bytes | ((u64)0x00020000u << 32); }
#define SUB4(v, i) __builtin_shufflevector(v, v, 4 * (i), 4 * (i) + 1, 4 * (i) + 2, 4 * (i) + 3)

__global__ __launch_bounds__(512, 2) void fused_kernel(
    char* a0, u64 a1, const u16* a2, u64 a3, const u16* a4, u64 a5, const u16* a6, u64 a7, const u16* a8, const u16* a9,
    const u16* a10, const u16* a11, const u16* a12, const u16* a13, const u16* a14, const u16* a15, const u16* a16, u16* a17) {
  __shared__ __attribute__((aligned(16))) char lds[L_END];
  u32x16 ka0; u64x8 ka1; u64x2 ka2;
  asm volatile("s_load_dwordx16 %0, %3, 0x0\n\ts_load_dwordx16 %1, %3, 0x40\n\ts_load_dwordx4 %2, %3, 0x80"
               : "=&s"(ka0), "=&s"(ka1), "=&s"(ka2) : "s"(__builtin_amdgcn_kernarg_segment_ptr()));
  SCHED();
  const u64 tstart = __builtin_amdgcn_s_memrealtime();
  u64 tprobe = tstart;
  char* wsp = nullptr;
  const int tid = threadIdx.x, lane = tid & 63;
  const int w = __builtin_amdgcn_readfirstlane(tid >> 6);
  const int b = blockIdx.x, g = b & 7, m = b >> 3, j = 32 * g + m, cg = (g + 1) & 7;
  u32x4 s4 = {SENT, SENT, SENT, SENT};

  u32 cwm = 0;
  if (CWARM && w == 7) {
    u64 pc;
    asm volatile("s_getpc_b64 %0" : "=s"(pc));
    const u32 co = (lane * 32 + m) * 128;
    if (co < CODE_BYTES) cwm = *(const __attribute__((address_space(1))) u32*)(pc + co);
    FENCE();
  }
  const u32 ldsb = (u32)(size_t)(LAS char*)lds;
  int kvo = ((tid >> 3) << 10) | ((((tid & 7) ^ ((tid >> 3) & 2) ^ ((tid >> 4) & 4)) ^ 4) << 4);
  u32 cls = OFF_E1 + cg * 262144 + m * 256, kvs = m * 65536 + cg * 128, kml = ldsb + L_KV + 1024 + w * 1024, vml = kml + 8192;
  int kho = (lane >= 40 && m > 0) ? ((kvo & 0x1fff) ^ 64) : OOB;
  int sko = lane < 32 ? lane * 16 : OOB;
  asm volatile("" : "+v"(kvo), "+v"(kho), "+v"(sko));
  asm volatile("" : "+s"(kvs), "+s"(kml), "+s"(vml));
  SCHED();
  asm volatile("s_waitcnt lgkmcnt(0)" : "+s"(ka0), "+s"(ka1), "+s"(ka2));
  asm volatile("s_mov_b32 m0, %3\n\ts_nop 0\n\tbuffer_load_dwordx4 %0, %1, %2 offen nt lds\n\t"
               "s_mov_b32 m0, %5\n\ts_nop 0\n\tbuffer_load_dwordx4 %0, %4, %2 offen nt lds"
               :: "v"(kvo), "s"(SUB4(ka0, 1)), "s"(kvs), "s"(kml), "s"(SUB4(ka0, 2)), "s"(vml) : "memory");
  if (w == 4)
    asm volatile("s_mov_b32 m0, %3\n\ts_nop 0\n\tbuffer_load_dwordx4 %0, %1, %2 offen nt lds"
                 :: "v"(kho), "s"(SUB4(ka0, 1)), "s"(m > 0 ? kvs - 8192 : 0u), "s"(ldsb + L_KV) : "memory");
  if (w == 5)
    asm volatile("s_mov_b32 m0, %3\n\ts_nop 0\n\tbuffer_load_dwordx4 %0, %1, %2 offen lds"
                 :: "v"(sko), "s"(SUB4(ka0, 3)), "s"(cg * 512), "s"(ldsb + L_KV + 17408) : "memory");
  SCHED();
  char* ws = (char*)(((u64)ka0[1] << 32) | ka0[0]);
  const u16 *gssmk = (const u16*)ka1[0], *gc = (const u16*)ka1[1], *gwmod = (const u16*)ka1[2], *gq = (const u16*)ka1[3],
            *gssmD = (const u16*)ka1[4], *gwr = (const u16*)ka1[5], *gwg = (const u16*)ka1[6], *gwu = (const u16*)ka1[7], *gwd = (const u16*)ka2[0];
  u16* gout = (u16*)ka2[1];
  wsp = ws;
  const rsrc_t rW = mkr(ws, WS_BYTES);
  const bool jobG = g == 7, jobF = g < 7 && m >= 16, jobM = g < 6 && m < 16;
  const int mcol = g * 512 + m * 32;
  int e1_out = cls + ((w & 3) * 8 + ((lane & 15) >> 1)) * 8192 + (w >> 2) * 128 + (lane & 1) * 64 + (lane >> 4) * 16;
  STAMP(16);
  if (w >= 6) {
    const int at = tid - 384;
    int zo = L_CNT + (at & 3) * 16;
    if (at < 60) zo = L_U + (at >= 30 ? 4608 - 480 : 0) + at * 16;
    if (at >= 64 && at < 80) zo = L_KC + ((at >> 1) & 7) * KC_STRIDE + (at & 1) * 16;
    if (at >= 80 && at < 84) zo = L_LIST + (at & 3) * 16;
    *(u32x4*)(lds + zo) = (u32x4){0, 0, 0, 0};
  }
  const int f0 = g * 256 + (m - 16) * 16;
  const int gd0 = m * 16;
  u32 cnt = ldsb + L_CNT;
  PIN(cnt);
  u64 ep;
#define EPOCH() asm volatile("s_load_dwordx2 %0, %1, %2\n\ts_waitcnt lgkmcnt(0)" : "=&s"(ep) : "s"(ws), "s"(OFF_EP) : "memory"); STAMP(18)
  u32x2 tagv;
#define MAKE_TAG() do { const u64 tag = ep + 1ull; tagv = (u32x2){(u32)tag, (u32)(tag >> 32)}; } while (0)
  u32x2 kx;
  u32 cw, dpair, pf0;
  if (w < 4) {
    const rsrc_t rS = mkr(gssmk, S * D * 2);
    const u32 dst = ldsb + L_KC + (w >> 1) * (4 * KC_STRIDE) + 32 + (w & 1) * 2048;
    dma16(rS, lane * 16, j * 8192 + w * 2048, dst);
    dma16(rS, lane * 16, j * 8192 + w * 2048 + 1024, dst + 1024);
  }
  {
    const int at = tid - 256;
    const u16* p0 = w < 4 ? gssmk : gwr;
    const u16* p1 = jobG ? gwd : gwmod;
    PINS(p0); PINS(p1);
    kx = __builtin_amdgcn_raw_buffer_load_b64(mkr(p0, S * D * 2), w < 4 ? j * 8192 + w * 2048 + 2048 : (at & 127) * 8, 0, 0);
    const int pf_g = m * 57344 + at * 128, pf_m = (m * 16 + (at >> 3)) * 6144 + g * 1024 + (at & 7) * 128;
    const u32 pf_n = jobG ? 192 : (g < 6 ? 128 : 0);
    pf0 = __builtin_amdgcn_raw_buffer_load_b32(mkr(p1, D * 6 * D * 2), (u32)at < pf_n ? (jobG ? pf_g : pf_m) : OOB, 0, PFAUX);
    FENCE();
    if (w >= 4) {
      cw = __builtin_amdgcn_raw_buffer_load_b32(mkr(gc, D * 2), jobM ? at * 4 : OOB, 0, 0);
      dpair = __builtin_amdgcn_raw_buffer_load_b32(mkr(gssmD, D * 2), j * 4, 0, 0);
    }
    FENCE();
  }
  EPOCH();
  MAKE_TAG();
  {
    const int blk = 4 * (w >> 2) + (lane >> 4) + 1;
    int ko = (blk * 8 + ((lane & 15) >> 2)) * 128 + (((w & 3) ^ ((lane >> 3) & 1) ^ ((blk & 1) << 1)) << 5) + (lane & 3) * 8;
    int cko = (lane & 17) == 0 ? OFF_CK + ((cg * 4 + (w & 3)) * 64 + m * 2 + (w >> 2)) * 64 + (lane & 14) * 4 + (lane >> 5) * 4 : OOB;
    u32 tw = lane < 32 ? tagv.x : tagv.y;
    PIN(ko); PIN(e1_out); PIN(cko); PIN(tw);
    int sko2 = L_KV + 17408 + ((w & 3) * 16 + (lane & 15)) * 8, ko2 = L_KV - 512 + (ko ^ 64);
    PIN(sko2); PIN(ko2);
    asm volatile("s_waitcnt vmcnt(4)\n\ts_barrier" ::: "memory");
    STAMP(20);
    const char* kp = lds + L_KV + ko;
    const u32x2 sk = *(const u32x2*)(lds + sko2);
    const u32x2 p0 = trread(lds + ko2), p1 = trread(kp), p2 = trread(kp + 512), v0 = trread(kp + 8192), v1 = trread(kp + 8704);
    const u32 c10 = __builtin_amdgcn_alignbyte(sk.x, sk.x, 2), c32 = __builtin_amdgcn_alignbyte(sk.y, sk.y, 2);
    const u32 c0z = sk.x & 0xffffu, cz3 = sk.y & 0xffff0000u, c21 = (sk.y & 0xffffu) | (sk.x & 0xffff0000u);
    const u32 P[6] = {p0.x, p0.y, p1.x, p1.y, p2.x, p2.y}, V[4] = {v0.x, v0.y, v1.x, v1.y};
    u32 o[4];
#pragma unroll
    for (int i = 0; i < 4; ++i) {
      const float ev = dot2(P[i + 2], c0z, dot2(P[i + 1], c21, dot2(P[i], cz3, 0.f)));
      const float od = dot2(P[i + 2], c10, dot2(P[i + 1], c32, 0.f));
      o[i] = pk(od * hi2f(V[i]), ev * lo2f(V[i]));
    }
    st128((u32x4){o[3], o[2], o[1], o[0]}, rW, e1_out);
    FENCE();
    {
      auto t = __builtin_amdgcn_permlane32_swap(fold2(o[3], o[2]), fold2(o[1], o[0]), false, false);
      u32 hs = t[0] + t[1];
      auto u = __builtin_amdgcn_permlane16_swap(hs, hs, false, false);
      hs = u[0] + u[1];
      hs += dppu<DPP_X1>(hs);
      __builtin_amdgcn_raw_buffer_store_b32(hs + tw, rW, cko, 0, SC1);
    }
    FENCE();
    STAMP(2);
  }
  const rsrc_t rO = mkr(gout, S * D * 2);

  if (w < 4) {
    const int ch = w >> 1, half = w & 1, r = lane & 15, h = lane >> 4;
    if (MPOS == 0 && jobM) signal(cnt, 12);
    int cki = OFF_CK + ((g * 4 + (m >> 3)) * 64 + w * 8 + (lane >> 3)) * 64 + (m & 7) * 8, e1i = OFF_E1 + j * 8192 + tid * 16;
    int o_z = OFF_E2 + j * 8192 + (128 * w + 4 * r + h) * 16;
    int o_cz = OFF_CZ + j * 2048 + w * 512 + r * 16 + h * 4;
    u32 tw2 = (h & 1) ? tagv.y : tagv.x;
    PIN(cki); PIN(e1i); PIN(o_z); PIN(o_cz); PIN(tw2);
    asm volatile("" : "+v"(kx), "+v"(pf0) :: "memory");
    STAMP(21);
    {
      char* kc = lds + L_KC + ch * (4 * KC_STRIDE) + (half * 128 + lane + 2) * 16;
      u32x4 kd[2];
      u32x2 kn[2];
#pragma unroll
      for (int i = 0; i < 2; ++i) { kd[i] = *(const u32x4*)(kc + i * 1024); kn[i] = *(const u32x2*)(kc + i * 1024 + 16); }
      if (lane == 63) kn[1] = kx;
#pragma unroll
      for (int i = 0; i < 2; ++i) {
        const u32 a01 = __builtin_amdgcn_alignbyte(kd[i].y, kd[i].x, 2), a12 = __builtin_amdgcn_alignbyte(kd[i].z, kd[i].y, 2);
        const u32 a23 = __builtin_amdgcn_alignbyte(kd[i].w, kd[i].z, 2), a34 = __builtin_amdgcn_alignbyte(kn[i].x, kd[i].w, 2);
        const u32 a45 = __builtin_amdgcn_alignbyte(kn[i].y, kn[i].x, 2);
        *(u32x4*)(kc + i * 1024 + KC_STRIDE) = (u32x4){a01, a12, a23, a34};
        *(u32x4*)(kc + i * 1024 + 2 * KC_STRIDE) = (u32x4){kd[i].y, kd[i].z, kd[i].w, kn[i].x};
        *(u32x4*)(kc + i * 1024 + 3 * KC_STRIDE) = (u32x4){a12, a23, a34, a45};
        if (i == 0 && half == 0 && lane == 0) {
          *(u32*)(kc - 4 + KC_STRIDE) = kd[0].x << 16;
          *(u32*)(kc - 4 + 2 * KC_STRIDE) = kd[0].x;
          *(u32x2*)(kc - 8 + 3 * KC_STRIDE) = (u32x2){kd[0].x << 16, a01};
        }
      }
    }
    STAMP(22);
    if (ch == 0) { if (arrive(cnt, 6) != 1) await<0>(cnt, 6, 2); } else { if (arrive(cnt, 7) != 1) await<0>(cnt, 7, 2); }
    STAMP(3);
    if (E1SLP < 0) drain(); else if (E1SLP) __builtin_amdgcn_s_sleep(E1SLP);
    STAMP(4);
    u32x4 x0 = ld128(rW, e1i, SC1), x1 = ld128(rW, e1i + 4096, SC1);
    u32x2 c0 = __builtin_amdgcn_raw_buffer_load_b64(rW, cki, 0, SC1), c1 = __builtin_amdgcn_raw_buffer_load_b64(rW, cki + 2048, 0, SC1);
    FENCE();
    if (MPOS == 2 && jobM) signal(cnt, 12);
    u32x2 Al[32], Ah[32];
    {
      const u32 ka = ldsb + L_KC + ch * (4 * KC_STRIDE) + ((1 + r) & 3) * KC_STRIDE + 2 * (128 * half + 8 * h + ((1 + r) & ~3));
#define RD4(i) asm volatile("ds_read_b64 %0, %8 offset:%9\n\tds_read_b64 %1, %8 offset:%9+8\n\tds_read_b64 %2, %8 offset:%9+64\n\t" \
      "ds_read_b64 %3, %8 offset:%9+72\n\tds_read_b64 %4, %8 offset:%9+128\n\tds_read_b64 %5, %8 offset:%9+136\n\t" \
      "ds_read_b64 %6, %8 offset:%9+192\n\tds_read_b64 %7, %8 offset:%9+200" \
      : "=&v"(Al[i]), "=&v"(Ah[i]), "=&v"(Al[8 + i]), "=&v"(Ah[8 + i]), "=&v"(Al[16 + i]), "=&v"(Ah[16 + i]), "=&v"(Al[24 + i]), "=&v"(Ah[24 + i]) \
      : "v"(ka), "i"(512 * i) : "memory")
      RD4(0); RD4(1); RD4(2); RD4(3); RD4(4); RD4(5); RD4(6); RD4(7);
      asm volatile("s_waitcnt lgkmcnt(0)" ::: "memory");
      PIN8(Al); PIN8(Al + 8); PIN8(Al + 16); PIN8(Al + 24); PIN8(Ah); PIN8(Ah + 8); PIN8(Ah + 16); PIN8(Ah + 24);
    }
    STAMP(5);
    {
      auto bad = [&]() __attribute__((always_inline)) {
        u32 h[4] = {fold2(x0.x, x0.y), fold2(x0.z, x0.w), fold2(x1.x, x1.y), fold2(x1.z, x1.w)};
#pragma unroll
        for (int i = 0; i < 4; ++i) h[i] += dppu<DPP_X1>(h[i]);
#pragma unroll
        for (int i = 0; i < 4; ++i) h[i] += dppu<DPP_X2>(h[i]);
#pragma unroll
        for (int i = 0; i < 4; ++i) h[i] += dppu<DPP_HM>(h[i]);
        return (((h[0] + tagv.x) ^ c0.x) | ((h[1] + tagv.y) ^ c0.y) | ((h[2] + tagv.x) ^ c1.x) | ((h[3] + tagv.y) ^ c1.y)) != 0;
      };
      bool nb = bad();
      RETRY(__any(nb)) {
        FENCE();
        if (nb) {
          x0 = ld128(rW, e1i, SC1); x1 = ld128(rW, e1i + 4096, SC1);
          c0 = __builtin_amdgcn_raw_buffer_load_b64(rW, cki, 0, SC1); c1 = __builtin_amdgcn_raw_buffer_load_b64(rW, cki + 2048, 0, SC1);
        }
        asm volatile("" : "+v"(x0), "+v"(x1), "+v"(c0), "+v"(c1));
        nb = bad();
      }
      STAMP(1);
      char* u0 = lds + L_U + ((tid >> 2) & 1) * 4608 + (30 + (tid >> 3) * 4 + (tid & 3)) * 16;
      *(u32x4*)(u0) = x0;
      *(u32x4*)(u0 + 2048) = x1;
    }
    if (arrive(cnt, 3) != 3) await<0>(cnt, 3, 4);
    STAMP(6);
    {
      f32x4 acc[8];
#pragma unroll
      for (int t = 0; t < 8; ++t) acc[t] = (f32x4){0, 0, 0, 0};
      const char* ub = lds + L_U + ch * 4608 + (30 + 2 * r + 1 - 16 * half - h) * 16;
      char* rp = lds + L_KC + w * 8192 + lane * 16;
      u32x4 B0[8], B1[8];
#pragma unroll
      for (int t = 0; t < 8; ++t) B0[t] = *(const u32x4*)(ub + 512 * t);
#pragma unroll
      for (int t = 0; t < 8; ++t) B1[t] = *(const u32x4*)(ub + 512 * t - 64);
      SCHED();
#define AF(ww, p) (u32x4){Al[8 * (ww) + (p)].x, Al[8 * (ww) + (p)].y, Ah[8 * (ww) + (p)].x, Ah[8 * (ww) + (p)].y}
#pragma unroll
      for (int bp = 0; bp < 8; ++bp) {
#pragma unroll
        for (int t = 7; t >= bp; --t) acc[t] = mfma(AF(0, t - bp), B0[bp], acc[t]);
        SCHED();
      }
#pragma unroll
      for (int bp = 0; bp < 8; ++bp) {
        B0[bp] = *(const u32x4*)(ub + 512 * bp - 128);
#pragma unroll
        for (int t = 7; t >= bp; --t) acc[t] = mfma(AF(1, t - bp), B1[bp], acc[t]);
        SCHED();
      }
#pragma unroll
      for (int bp = 0; bp < 8; ++bp) {
        B1[bp] = *(const u32x4*)(ub + 512 * bp - 192);
#pragma unroll
        for (int t = 7; t >= bp; --t) acc[t] = mfma(AF(2, t - bp), B0[bp], acc[t]);
        SCHED();
      }
#pragma unroll
      for (int t = 0; t < 8; ++t) {
#pragma unroll
        for (int bp = 0; bp <= t; ++bp) {
          acc[t] = mfma(AF(3, t - bp), B1[bp], acc[t]);
          if (t >= 2 && bp == 1) { SCHED(); *(f32x4*)(rp + (t - 1) * 1024) = acc[t - 1]; if (t == 2) *(f32x4*)(rp) = acc[0]; SCHED(); }
        }
        SCHED();
      }
      *(f32x4*)(rp + 7 * 1024) = acc[7];
    }
    STAMP(7);
    if (arrive(cnt, 14) != 7) await<0>(cnt, 14, 8);
    u32x4 zt[2];
#pragma unroll
    for (int t = 0; t < 2; ++t) {
      const char* rp = lds + L_KC + (2 * w + t) * 1024 + lane * 16;
      const char* dp = lds + L_KD + (2 * w + t) * 1024 + lane * 16;
      const f32x4 ya = *(const f32x4*)(rp) + *(const f32x4*)(rp + 8192) + *(const f32x4*)(dp);
      const f32x4 yb = *(const f32x4*)(rp + 16384) + *(const f32x4*)(rp + 24576) + *(const f32x4*)(dp + 8192);
      zt[t] = (u32x4){pk(ya[0], yb[0]), pk(ya[1], yb[1]), pk(ya[2], yb[2]), pk(ya[3], yb[3])};
      st128(zt[t], rW, o_z + 1024 * t);
    }
    FENCE();
#pragma unroll
    for (int t = 0; t < 2; ++t) {
      auto u = __builtin_amdgcn_permlane16_swap(fold2(zt[t].x, zt[t].y), fold2(zt[t].z, zt[t].w), false, false);
      __builtin_amdgcn_raw_buffer_store_b32(u[0] + u[1] + tw2, rW, o_cz + 256 * t, 0, SC1);
    }
    signal(cnt, 9);
    STAMP(15);
    u32x4 Wf[14];
    const int wvo = jobG ? (w * 448 + (lane >> 1)) * 1024 + gd0 * 2 + (lane & 1) * 16 : (w * 128 + (lane >> 1)) * (F * 2) + f0 * 2 + (lane & 1) * 16;
    if (F1POS == 0 && jobF) {
#pragma unroll
      for (int i = 0; i < 4; ++i) dmaw(mkr(gwg, D * F * 2), wvo, i * 32 * F * 2, ldsb + L_W + w * 4096 + i * 1024);
    } else if (jobG) {
#pragma unroll
      for (int i = 0; i < 4; ++i) dmaw(mkr(gwd, F * D * 2), wvo, i * 32 * 1024, ldsb + L_W + w * 14336 + i * 1024);
    }
    __builtin_amdgcn_s_sleep(ZPACE);
    STAMP(8);
    {
      const int zo = OFF_E2 + (tid >> 1) * 8192 + j * 32 + (tid & 1) * 16, zc = OFF_CZ + (tid >> 1) * 2048 + j * 8 + (tid & 1) * 4;
      u32x4 z0 = ld128(rW, zo, SC1), z1 = ld128(rW, zo + 128 * 8192, SC1);
      u32 k0 = __builtin_amdgcn_raw_buffer_load_b32(rW, zc, 0, SC1), k1 = __builtin_amdgcn_raw_buffer_load_b32(rW, zc + 128 * 2048, 0, SC1);
      const u32 tw3 = (lane & 1) ? tagv.y : tagv.x;
      auto bad = [&]() __attribute__((always_inline)) {
        const u32 a0 = fold2(z0.x, z0.y), a1 = fold2(z0.z, z0.w), b0 = fold2(z1.x, z1.y), b1 = fold2(z1.z, z1.w);
        const u32 sa0 = a0 + dppu<DPP_X1>(a0), sa1 = a1 + dppu<DPP_X1>(a1), sb0 = b0 + dppu<DPP_X1>(b0), sb1 = b1 + dppu<DPP_X1>(b1);
        u32 d = ((((lane & 1) ? sa1 : sa0) + tw3) ^ k0) | ((((lane & 1) ? sb1 : sb0) + tw3) ^ k1);
        return d;
      };
      u32 nb = bad();
      STAMP(17);
      RETRY(__any(nb != 0)) {
        FENCE();
        nb |= dppu<DPP_X1>(nb);
        if (nb) {
          z0 = ld128(rW, zo, SC1); z1 = ld128(rW, zo + 128 * 8192, SC1);
          k0 = __builtin_amdgcn_raw_buffer_load_b32(rW, zc, 0, SC1); k1 = __builtin_amdgcn_raw_buffer_load_b32(rW, zc + 128 * 2048, 0, SC1);
        }
        asm volatile("" : "+v"(z0), "+v"(z1), "+v"(k0), "+v"(k1));
        nb = bad();
      }
      STAMP(9);
      char* zp = lds + L_Z + (tid & 1) * 4160 + (tid >> 1) * 4;
#pragma unroll
      for (int e = 0; e < 4; ++e) { *(u32*)(zp + e * 1040) = z0[e]; *(u32*)(zp + e * 1040 + 512) = z1[e]; }
    }
    bar();
    if (!(jobF || jobG)) { FLUSH(); return; }
    await<2>(cnt, 10, 4);
    if (jobG) {
#pragma unroll
      for (int i = 4; i < 8; ++i) dmaw(mkr(gwd, F * D * 2), wvo, i * 32 * 1024, ldsb + L_W + w * 14336 + i * 1024);
    } else {
#pragma unroll
      for (int i = 0; i < 4; ++i) dmaw(mkr(gwu, D * F * 2), wvo, i * 32 * F * 2, ldsb + L_W + 16384 + w * 4096 + i * 1024);
    }
    __builtin_amdgcn_s_sleep(KPACE);
    STAMP(11);
    const u64 tb6 = __builtin_amdgcn_s_memrealtime();
    const int ko = OFF_RK + g * 256 * RKS + (tid >> 1) * RKS + (tid & 1) * 16;
    u32x4 ka = ld128(rW, ko, SC1), kb = ld128(rW, ko + 128 * RKS, SC1);
    if (jobF) {
      asm volatile("s_waitcnt vmcnt(2)" ::: "memory");
      const char* wp = lds + L_W + w * 4096 + h * 256 + r * 8;
#pragma unroll
      for (int i = 0; i < 8; ++i) Wf[i] = trfrag(wp + (i >> 2) * 16384 + (i & 3) * 1024, 128);
      PIN8(Wf);
    }
    asm volatile("" : "+v"(ka), "+v"(kb) :: "memory");
    u32 mx = max(umax4(ka), umax4(kb));
    RETRY(!__all(mx != SENT)) {
      FENCE();
      if (mx == SENT) { ka = ld128(rW, ko, SC1); kb = ld128(rW, ko + 128 * RKS, SC1); mx = max(umax4(ka), umax4(kb)); }
    }
    if (jobG) {
#pragma unroll
      for (int i = 8; i < 14; ++i) dmaw(mkr(gwd, F * D * 2), wvo, i * 32 * 1024, ldsb + L_W + w * 14336 + i * 1024);
    }
    STAMP(12);
    u32 gm = max(mx, dppu<DPP_X1>(mx));
    gm = max(gm, dppu<DPP_X2>(gm)); gm = max(gm, dppu<DPP_HM>(gm)); gm = max(gm, dppu<DPP_M>(gm));
    if (r == 0) *(u32*)(lds + L_GM + (4 * w + h) * 4) = gm;
    const u32 ky[8] = {ka.x, ka.y, ka.z, ka.w, kb.x, kb.y, kb.z, kb.w};
    u32 m2 = 0;
#pragma unroll
    for (int e = 0; e < 8; ++e) m2 = max(m2, ky[e] == mx ? 0u : ky[e]);
    PIN(m2);
    bar();
    u32 mine;
    u32 rk = rank16(lds + L_GM, lane, mine);
    const u32 T = __builtin_amdgcn_readlane(mine, __builtin_ctzll(__ballot(rk == 7)));
    if (mx >= T) {
      const u32 pos = __hip_atomic_fetch_add((u32*)(lds + L_CNT), 1u, __ATOMIC_RELAXED, __HIP_MEMORY_SCOPE_WORKGROUP);
      *(u32*)(lds + L_LIST + pos * 4) = mx;
    }
    if (__any(m2 >= T)) {
#pragma unroll
      for (int e = 0; e < 8; ++e)
        if (ky[e] >= T && ky[e] != mx) {
          const u32 pos = __hip_atomic_fetch_add((u32*)(lds + L_CNT), 1u, __ATOMIC_RELAXED, __HIP_MEMORY_SCOPE_WORKGROUP);
          *(u32*)(lds + L_LIST + pos * 4) = ky[e];
        }
    }
    bar();
    u32 cand = *(const u32*)(lds + L_LIST + r * 4);
    const u32 N = peek(cnt, 0);
    if (N > 16) {
      for (u32 i = tid; i < N; i += 256) {
        const u32 kx = *(const u32*)(lds + L_LIST + i * 4);
        u32 c = 0;
#pragma unroll 1
        for (u32 q = 0; q < N; ++q) c += *(const u32*)(lds + L_LIST + q * 4) > kx;
        if (c < 8) *(u32*)(lds + L_SEL + c * 4) = kx;
      }
      if (arrive(cnt, 15) != 3) await<0>(cnt, 15, 4);
      if (tid < 16) *(u32*)(lds + L_LIST + tid * 4) = tid < 8 ? *(const u32*)(lds + L_SEL + tid * 4) : 0u;
      if (arrive(cnt, 15) != 7) await<0>(cnt, 15, 8);
      cand = *(const u32*)(lds + L_LIST + r * 4);
    }
    STAMP(13);
    if (jobF) {
      const int ho = OFF_H2 + (2047 - (int)(cand & 0x7ffu)) * 1024 + (w * 128 + 8 * h) * 2;
      u32x4 a[4];
#pragma unroll
      for (int ks = 0; ks < 4; ++ks) a[ks] = ld128(rW, ho + ks * 64, SC1);
      FENCE();
      rk = rank16(lds + L_LIST, lane, mine);
      if (w == 0 && lane < 16 && rk < 8) *(u32*)(lds + L_RANK + rk * 4) = (lane >> 2) * 256 + (lane & 3) * 4;
      RETRY(!__all(max(max(umax4(a[0]), umax4(a[1])), max(umax4(a[2]), umax4(a[3]))) != SENT)) {
        FENCE();
#pragma unroll
        for (int ks = 0; ks < 4; ++ks) a[ks] = ld128(rW, ho + ks * 64, SC1);
        PIN(a[0]); PIN(a[1]); PIN(a[2]); PIN(a[3]);
      }
      f32x4 ag = {0, 0, 0, 0}, au = {0, 0, 0, 0};
#pragma unroll
      for (int ks = 0; ks < 4; ++ks) { ag = mfma(a[ks], Wf[ks], ag); au = mfma(a[ks], Wf[4 + ks], au); }
      *(f32x4*)(lds + L_KC + w * 2048 + lane * 16) = ag;
      *(f32x4*)(lds + L_KC + w * 2048 + 1024 + lane * 16) = au;
      bar();
      STAMP(14);
      if (w < 2) {
        const char* p = lds + L_KC + *(const u32*)(lds + L_RANK + (lane >> 3) * 4) + (8 * w + (lane & 7)) * 16;
        float gv = 0, uv = 0;
#pragma unroll
        for (int i = 0; i < 4; ++i) { gv += *(const float*)(p + i * 2048); uv += *(const float*)(p + i * 2048 + 1024); }
        gv = rb(gv);
        const float v = rb(gv * __builtin_amdgcn_rcpf(1.f + __expf(-gv))) * rb(uv);
        const float vn = dppf<DPP_X1>(v);
        __builtin_amdgcn_raw_buffer_store_b32(nos(pk(v, vn)), rW, (lane & 1) ? OOB : OFF_GU + f0 * 16 + w * 128 + lane * 2, 0, SC1);
        STAMP(10);
      }
    } else {
      rk = rank16(lds + L_LIST, lane, mine);
      if (w == 0 && lane < 16 && rk < 8) *(u32*)(lds + L_SEL + rk * 4) = mine;
      bar();
      drain();
      {
        const char* wp = lds + L_W + w * 14336 + h * 256 + r * 8;
#pragma unroll
        for (int i = 0; i < 14; ++i) Wf[i] = trfrag(wp + i * 1024, 128);
        PIN8(Wf); PIN8(Wf + 6);
      }
      while (__builtin_amdgcn_s_memrealtime() < tb6 + GDELAY) __builtin_amdgcn_s_sleep(1);
      STAMP(14);
      const int go = OFF_GU + (w * 56 + 4 * (r >> 3) + h) * 128 + (r & 7) * 16;
      u32x4 a[7];
#pragma unroll 1
      for (int spins = 0; spins < SPIN_CAP; ++spins) {
        FENCE();
        u32 mm = 0;
#pragma unroll
        for (int i = 0; i < 7; ++i) a[i] = ld128(rW, go + i * 1024, SC1);
#pragma unroll
        for (int i = 0; i < 7; ++i) mm = max(mm, umax4(a[i]));
        if (__all(mm != SENT)) break;
      }
      STAMP(10);
      f32x4 a0 = {0, 0, 0, 0}, a1 = {0, 0, 0, 0};
#pragma unroll
      for (int i = 0; i < 7; ++i) { a0 = mfma(a[i], Wf[2 * i], a0); a1 = mfma(a[i], Wf[2 * i + 1], a1); }
      *(f32x4*)(lds + L_KC + w * 1024 + lane * 16) = lane < 32 ? a0 : a1;
      bar();
    }
    FLUSH();
  } else {
    const int a = w - 4, at = tid - 256, hl = lane & 31, row = 8 * j + 2 * a + (lane >> 5);
    __builtin_amdgcn_s_setprio(1);
    int o_own = j * 8192 + at * 16;
    int o_m = (a * 128 + (lane >> 2)) * 6144 + mcol * 2 + (lane & 3) * 16;
    int o_w = L_W + a * 8192 + ((lane >> 4) * 8 + ((lane & 15) >> 2)) * 64 + (lane & 3) * 8;
    u32x2 wrw = kx;
    asm volatile("" : "+v"(o_own), "+v"(o_m), "+v"(o_w), "+v"(s4), "+v"(dpair), "+v"(cw), "+v"(wrw), "+v"(cwm), "+v"(pf0) :: "memory");
    u32x4 av[4];
    auto mod_request = [&]() __attribute__((always_inline)) {
      const rsrc_t rM = mkr(gwmod, D * 6 * D * 2);
#pragma unroll
      for (int i = 0; i < 8; ++i) dmaw(rM, o_m, i * 16 * 6144, ldsb + L_W + a * 8192 + i * 1024);
    };
    auto mod_compute = [&]() __attribute__((always_inline)) {
      STAMP(21);
      u32x4 bw[8];
#pragma unroll
      for (int i = 0; i < 8; ++i) bw[i] = trfrag(lds + o_w + (i >> 1) * 2048 + (i & 1) * 32, 256);
      PIN8(bw);
      f32x4 d0 = {0, 0, 0, 0}, d1 = {0, 0, 0, 0};
#pragma unroll
      for (int ks = 0; ks < 4; ++ks) { d0 = mfma(av[ks], bw[2 * ks], d0); d1 = mfma(av[ks], bw[2 * ks + 1], d1); }
      if (lane < 16) { *(float*)(lds + L_MP + a * 128 + lane * 4) = d0[0]; *(float*)(lds + L_MP + a * 128 + 64 + lane * 4) = d1[0]; }
      const u32 nth = arrive(cnt, 2);
      {
        f32x4 pp[4];
#pragma unroll
        for (int i = 0; i < 4; ++i) pp[i] = *(const f32x4*)(lds + L_MP + i * 128 + (lane & 7) * 16);
        float sm[4];
#pragma unroll
        for (int e = 0; e < 4; ++e) sm[e] = add1(add1(pp[0][e], pp[1][e]), add1(pp[2][e], pp[3][e]));
        const u32 p0 = pk(sm[0], sm[1]), p1 = pk(sm[2], sm[3]);
        st128((u32x4){p0, p1, p0 ^ tagv.x, p1 ^ tagv.y}, rW, nth == 3 ? OFF_MOD + (lane >> 3) * 12288 + mcol * 4 + (lane & 7) * 16 : OOB);
        FENCE();
      }
      STAMP(3);
    };
    if (jobM) {
      if (MPOS == 0) { await<1>(cnt, 12, 4); mod_request(); }
      f32x2 cv = up2(cw);
      cv.x = mul1(cv.x, __builtin_amdgcn_rcpf(add1(1.f, __builtin_amdgcn_exp2f(mul1(cv.x, -1.44269504088896f)))));
      cv.y = mul1(cv.y, __builtin_amdgcn_rcpf(add1(1.f, __builtin_amdgcn_exp2f(mul1(cv.y, -1.44269504088896f)))));
      *(u32*)(lds + L_SC + at * 4) = pk2(cv);
#pragma unroll
      for (int ks = 0; ks < 4; ++ks) av[ks] = *(const u32x4*)(lds + L_SC + a * 256 + ks * 64 + (lane >> 4) * 16);
      PIN(av[0]); PIN(av[1]); PIN(av[2]); PIN(av[3]);
      if (MPOS == 2) { await<1>(cnt, 12, 4); mod_request(); }
      if (MPOS == 0 || MPOS == 2) { drain(); mod_compute(); }
    }
    int o_sm = OOB, o_q = row * 1024 + hl * 16;
    if (at < 16) o_sm = OFF_RK + (at >> 1) * 256 * RKS + j * RKS + (at & 1) * 16;
    if (jobF && at >= 16 && at < 32) o_sm = OFF_GU + f0 * 16 + (at - 16) * 16;
    int o_pf = jobG ? m * 57344 + 24576 + at * 128 : (a < 2 ? (m * 16 + (lane >> 2)) * (F * 2) + g * 512 + (lane & 3) * 128 : OOB);
    const int un = 128 * a + 4 * (lane & 15) + (lane >> 4);
    int o_u = L_U + (30 + (un >> 1)) * 16 + ((un & 1) ? 0 : 8);
    int o_mod = OFF_MOD + g * 12288 + (a >> 1) * 6144 + (at & 127) * 16;
    asm volatile("" : "+v"(o_sm), "+v"(o_q), "+v"(o_pf), "+v"(o_u), "+v"(o_mod));
    await<1>(cnt, 3, 4);
    STAMP(6);
    PIN(pf0);
    if (MPOS == 1 && jobM) { mod_request(); if (arrive(cnt, 12) != 3) await<1>(cnt, 12, 4); }
    __builtin_amdgcn_raw_buffer_store_b128(s4, rW, o_own, OFF_H2, SC1);
    __builtin_amdgcn_raw_buffer_store_b128(s4, rW, o_own, OFF_H2 + 4096, SC1);
    st128(s4, rW, o_sm);
    FENCE();
    u32x4 q0 = ld128(mkr(gq, S * D * 2), o_q, NT), q1 = ld128(mkr(gq, S * D * 2), o_q + 512, NT);
    FENCE();
    u32 pf1 = __builtin_amdgcn_raw_buffer_load_b32(mkr(jobG ? gwd : (a == 0 ? gwg : gwu), D * F * 2), o_pf, 0, PFAUX);
    FENCE();
    {
      const f32x2 dv = up2(dpair);
#pragma unroll
      for (int t = 0; t < 2; ++t) {
        const u32x2 ua = *(const u32x2*)(lds + o_u + 512 * t), ub = *(const u32x2*)(lds + o_u + 512 * t + 4608);
        char* dp = lds + L_KD + (2 * a + t) * 1024 + lane * 16;
        *(f32x4*)(dp) = (f32x4){mul0(hi2f(ua.y), dv.x), mul0(lo2f(ua.y), dv.x), mul0(hi2f(ua.x), dv.x), mul0(lo2f(ua.x), dv.x)};
        *(f32x4*)(dp + 8192) = (f32x4){mul0(hi2f(ub.y), dv.y), mul0(lo2f(ub.y), dv.y), mul0(hi2f(ub.x), dv.y), mul0(lo2f(ub.x), dv.y)};
      }
    }
    if (MPOS == 1 && jobM) {
      asm volatile("s_waitcnt vmcnt(6)" ::: "memory");
      mod_compute();
      asm volatile("s_waitcnt vmcnt(4)" ::: "memory");
    } else {
      asm volatile("s_waitcnt vmcnt(3)" ::: "memory");
    }
    signal(cnt, 14);
    STAMP(5);
    asm volatile("" : "+v"(q0), "+v"(q1) :: "memory");
    STAMP(7);
    __builtin_amdgcn_s_setprio(0);
    f32x2 x[8], gah[8];
    {
      unpack16(q0, q1, x);
      float s = 0;
#pragma unroll
      for (int e = 0; e < 8; ++e) s = add0(s, add0(x[e].x, x[e].y));
      const float mean = half_sum(s) * (1.f / 512.f);
      float qq = 0;
#pragma unroll
      for (int e = 0; e < 8; ++e) { const float d0 = add0(x[e].x, -mean), d1 = add0(x[e].y, -mean); qq = fma0(d0, d0, qq); qq = fma0(d1, d1, qq); }
      const float var = rb(half_sum(qq) * (1.f / 512.f));
      float isd = __builtin_amdgcn_rcpf(rb(__builtin_amdgcn_sqrtf(rb(var + 1e-5f))));
      float mi = -rb(mean) * isd;
      PIN(isd); PIN(mi);
#pragma unroll
      for (int e = 0; e < 8; ++e) gah[e] = (f32x2){fma0(x[e].x, isd, mi), fma0(x[e].y, isd, mi)};
      PIN8(x); PIN8(gah);
    }
    STAMP(8);
    asm volatile("" : "+v"(pf1) :: "memory");
    if (F1POS == 1 && jobF) {
#pragma unroll
      for (int i = 0; i < 4; ++i) dmaw(mkr(gwg, D * F * 2), (a * 128 + (lane >> 1)) * (F * 2) + f0 * 2 + (lane & 1) * 16, i * 32 * F * 2, ldsb + L_W + a * 4096 + i * 1024);
    }
    STAMP(4);
    if (MRD == 0) await<1>(cnt, 9, 4);
    if (MODSLP) __builtin_amdgcn_s_sleep(MODSLP);
    {
      u32x4 mu[3];
#pragma unroll
      for (int i = 0; i < 3; ++i) mu[i] = ld128(rW, o_mod + i * 2048, SC1);
      auto modbad = [&]() __attribute__((always_inline)) {
        u32 d = 0;
#pragma unroll
        for (int i = 0; i < 3; ++i) d |= (mu[i].x ^ mu[i].z ^ tagv.x) | (mu[i].y ^ mu[i].w ^ tagv.y);
        return d != 0;
      };
      bool nb = modbad();
      RETRY(__any(nb)) {
        FENCE();
        __builtin_amdgcn_s_sleep(3);
        if (nb) {
#pragma unroll
          for (int i = 0; i < 3; ++i) mu[i] = ld128(rW, o_mod + i * 2048, SC1);
        }
        asm volatile("" : "+v"(mu[0]), "+v"(mu[1]), "+v"(mu[2]));
        nb = modbad();
      }
      __builtin_amdgcn_s_setprio(1);
      STAMP(17);
      const f32x2 s0 = up2(mu[0].x), s1 = up2(mu[0].y), c0 = rb2(up2(mu[1].x) + 1.f), c1 = rb2(up2(mu[1].y) + 1.f), g0 = up2(mu[2].x), g1 = up2(mu[2].y);
      char* mo = lds + L_MOD + (at & 1) * 1024 + ((at & 127) >> 1) * 16;
      auto put = [&](int v, f32x2 lo, f32x2 hi) __attribute__((always_inline)) { *(f32x4*)(mo + v * 2048) = (f32x4){lo.x, lo.y, hi.x, hi.y}; };
      if (a < 2) {
        put(0, g0 * c0, g1 * c1);
        put(1, g0 * s0, g1 * s1);
      } else {
        const f32x2 w0 = up2(wrw.x), w1 = up2(wrw.y), r0 = c0 * w0, r1 = c1 * w1, sa_ = r0 + r1, sc_ = s0 * w0 + s1 * w1;
        put(2, r0, r1); put(3, c0, c1); put(4, s0, s1); put(5, g0, g1);
        const float pa = half_sum(sa_.x + sa_.y), pc2 = half_sum(sc_.x + sc_.y);
        if (hl == 0) *(f32x2*)(lds + L_MOD + 12288 + ((a & 1) * 2 + (lane >> 5)) * 8) = (f32x2){pa, pc2};
      }
      STAMP(9);
      if (arrive(cnt, 13) != 3) await<1>(cnt, 13, 4);
    }
    f32x2 ra[8], cb1[8], sb[8], gb[8];
    float suma, sumc;
    {
      const char* mp = lds + L_MOD + hl * 16;
      auto vec16 = [&](int v, f32x2* o) __attribute__((always_inline)) {
#pragma unroll
        for (int i = 0; i < 4; ++i) {
          const f32x4 t = *(const f32x4*)(mp + v * 2048 + (i >> 1) * 512 + (i & 1) * 1024);
          o[2 * i] = (f32x2){t[0], t[1]}; o[2 * i + 1] = (f32x2){t[2], t[3]};
        }
      };
      f32x2 a1[8], a0[8];
      vec16(0, a1); vec16(1, a0); vec16(2, ra); vec16(3, cb1); vec16(4, sb); vec16(5, gb);
      const f32x4 pa = *(const f32x4*)(lds + L_MOD + 12288), pc2 = *(const f32x4*)(lds + L_MOD + 12288 + 16);
#pragma unroll
      for (int e = 0; e < 8; ++e) gah[e] = gah[e] * a1[e] + a0[e];
      suma = (pa[0] + pa[2]) + (pc2[0] + pc2[2]);
      sumc = (pa[1] + pa[3]) + (pc2[1] + pc2[3]);
      PIN8(gah); PIN8(ra); PIN8(cb1); PIN8(sb); PIN8(gb); PIN(suma); PIN(sumc);
    }
    int o_key = hl < 8 ? OFF_RK + hl * 256 * RKS + j * RKS + (row & 7) * 4 : OOB;
    PIN(o_key);
    STAMP(10);
    bar();
    STAMP(11);
    {
      f32x2 z[8];
      const char* zr = lds + L_Z + (2 * a + (lane >> 5)) * 1040 + hl * 16;
      unpack16(*(const u32x4*)(zr), *(const u32x4*)(zr + 512), z);
      f32x2 s1 = {0, 0}, s2 = {0, 0}, s3 = {0, 0};
#pragma unroll
      for (int e = 0; e < 8; ++e) { x[e] += gah[e] * z[e]; s1 += x[e]; s2 += x[e] * x[e]; s3 += x[e] * ra[e]; }
      const float t1 = half_sum(s1.x + s1.y), t2 = half_sum(s2.x + s2.y), t3 = half_sum(s3.x + s3.y);
      const float mean = t1 * (1.f / 512.f);
      const f32x2 mvb = rb2((f32x2){mean, fmaxf(t2 * (1.f / 512.f) - mean * mean, 0.f)});
      const float mb = mvb.x, isd = __builtin_amdgcn_rcpf(rb(__builtin_amdgcn_sqrtf(rb(mvb.y + 1e-5f))));
      const float rr = (t3 - mb * suma) * isd + sumc;
      u32 kb = pk(0.f, rr);
      kb ^= ((int)kb < 0) ? 0xffff0000u : 0x80000000u;
      __builtin_amdgcn_raw_buffer_store_b32(kb | 0x8000u | (u32)(2047 - row), rW, o_key, 0, SC1);
      signal(cnt, 10);
      STAMP(12);
      u32 hw[8], ow[8];
#pragma unroll
      for (int e = 0; e < 8; ++e) {
        const f32x2 h2 = (x[e] - mb) * isd * cb1[e] + sb[e];
        hw[e] = nos(pk2(h2));
        ow[e] = pk2(x[e] + gb[e] * h2);
      }
      st128((u32x4){hw[0], hw[1], hw[2], hw[3]}, rW, OFF_H2 + row * 1024 + hl * 16);
      st128((u32x4){hw[4], hw[5], hw[6], hw[7]}, rW, OFF_H2 + row * 1024 + 512 + hl * 16);
      PIN8(ow);
      STAMP(13);
      if (jobF || jobG) bar(); else __builtin_amdgcn_s_sleep(30);
      st128((u32x4){ow[0], ow[1], ow[2], ow[3]}, rO, row * 1024 + hl * 16);
      st128((u32x4){ow[4], ow[5], ow[6], ow[7]}, rO, row * 1024 + 512 + hl * 16);
    }
    if (b == 0 && a == 0) __builtin_amdgcn_raw_buffer_store_b64(tagv, rW, lane == 0 ? OFF_EP : OOB, 0, SC1);
    if (jobF || jobG) bar();
    drain();
    STAMP(14);
    if (arrive(cnt, 5) == 3) __builtin_amdgcn_raw_buffer_store_b64(tagv, rW, lane == 0 ? OFF_FL3 + j * 128 : OOB, 0, SC1);
    if (jobG && a < 2) {
      __builtin_amdgcn_s_setprio(0);
      bar();
      const int r = lane & 15, tok = at >> 4;
      const u32 key = *(const u32*)(lds + L_SEL + tok * 4);
      const int orow = 2047 - (int)(key & 0x7ffu);
#pragma unroll 1
      for (int spins = 0; spins < SPIN_CAP; ++spins) {
        FENCE();
        const u32x2 f = __builtin_amdgcn_raw_buffer_load_b64(rW, OFF_FL3 + (orow >> 3) * 128, 0, SC1);
        if (__all(f.x == tagv.x && f.y == tagv.y)) break;
        __builtin_amdgcn_s_sleep(4);
      }
      FENCE();
      float oold = lo2f(__builtin_amdgcn_raw_buffer_load_b16(rO, orow * 1024 + (gd0 + r) * 2, 0, SC1));
      u32 rbits = key >> 16;
      rbits = (rbits & 0x8000u) ? (rbits & 0x7fffu) : (rbits ^ 0xffffu);
      float gy = rb(__builtin_amdgcn_rcpf(1.f + __expf(-lo2f(rbits)))) * *(const float*)(lds + L_MOD + 10240 + ((r >> 2) & 1) * 1024 + ((gd0 + r) >> 3) * 16 + (r & 3) * 4);
      int oo = (r & 1) ? OOB : orow * 1024 + (gd0 + r) * 2, po = L_KC + ((tok >> 2) * 16 + r) * 16 + (tok & 3) * 4;
      PIN(gy); PIN(oold); PIN(oo); PIN(po);
      STAMP(15);
      bar();
      float y = 0;
#pragma unroll
      for (int i = 0; i < 8; ++i) y += *(const float*)(lds + po + i * 512);
      const float o = oold + gy * rb(y);
      const float on = dppf<DPP_X1>(o);
      __builtin_amdgcn_raw_buffer_store_b32(pk(o, on), rO, oo, 0, SC1);
      STAMP(16);
      {
        const u64 tend = __builtin_amdgcn_s_memrealtime();
        u32 mx = 0, mn2 = 0xffffffffu, mx2 = 0;
        for (int i = 0; i < 4; ++i) { const u32x2 v = __builtin_amdgcn_raw_buffer_load_b64(rW, OFF_ST + (lane + 64 * i) * 8, 0, SC1); const u32 d = (u32)tend - v.x, e = (u32)tend - v.y; mx = max(mx, d); mn2 = min(mn2, e); mx2 = max(mx2, e); }
        for (int o = 32; o; o >>= 1) { mx = max(mx, (u32)__shfl_xor((int)mx, o)); mn2 = min(mn2, (u32)__shfl_xor((int)mn2, o)); mx2 = max(mx2, (u32)__shfl_xor((int)mx2, o)); }
        u32 amt = PK == 0 ? (mx >> 30) : (PK == 2 ? mx : (PK == 3 ? mx - mn2 : mx - mx2));
        amt = __builtin_amdgcn_readfirstlane(min(amt, 3000u));
        const u64 until = __builtin_amdgcn_s_memrealtime() + 4 * (u64)amt;
        while (__builtin_amdgcn_s_memrealtime() < until) __builtin_amdgcn_s_sleep(2);
      }

    }
    FLUSH();
  }
}

extern "C" void kernel_launch(void* const* d_in, const int* in_sizes, int n_in, void* d_out, int out_size, void* d_ws,
                              size_t ws_size, hipStream_t stream) {
  fused_kernel<<<256, 512, 0, stream>>>((char*)d_ws + (WSEND ? ((ws_size - WS_BYTES) & ~(size_t)4095) : 0), desc_hi(WS_BYTES), (const u16*)d_in[1], desc_hi(S * D * 2), (const u16*)d_in[2],
                                        desc_hi(S * D * 2), (const u16*)d_in[3], desc_hi(D * 8), (const u16*)d_in[4], (const u16*)d_in[11],
                                        (const u16*)d_in[12], (const u16*)d_in[0], (const u16*)d_in[5], (const u16*)d_in[7],
                                        (const u16*)d_in[8], (const u16*)d_in[9], (const u16*)d_in[10], (u16*)d_out);
}
```

```cpp
#include <hip/hip_runtime.h>
#include <stdint.h>

typedef unsigned short u16;
typedef unsigned int u32;
typedef unsigned long long u64;
typedef short bf16x8 __attribute__((ext_vector_type(8)));
typedef short s16x4 __attribute__((ext_vector_type(4)));
typedef float f32x4 __attribute__((ext_vector_type(4)));
typedef float f32x2 __attribute__((ext_vector_type(2)));
typedef __bf16 bf16x2 __attribute__((ext_vector_type(2)));
typedef u32 u32x4 __attribute__((ext_vector_type(4)));
typedef u32 u32x2 __attribute__((ext_vector_type(2)));
typedef __amdgpu_buffer_rsrc_t rsrc_t;
#define LAS __attribute__((address_space(3)))

constexpr int S = 2048, D = 512, F = 1792;
constexpr u32 SENT = 0xFFFFFFFFu;
constexpr int SC1 = 16, NT = 2;
constexpr int SPIN_CAP = 1 << 14;
#define RKS 32
#define JOBPOS 0
#define FW0 4
#define CHKSLP 4
#define ZSLP 0
#define MPOS 0
#define CODE_BYTES 23000
#define ZPACE 8
#define KPACE 6
#define CWARM 1
#define PFAUX 0
#define WNT 1
#define E1SLP 0
#define MRD 1
#define PFPOS 0
#define F1POS 0
#define WSEND 0
#define KSLP 0
#define MODSLP 0
#define F2S 8
#define SSMPOS 0
#define F1S 8
#define PF0SLP 0
#define GDELAY 200

constexpr int OFF_E1 = 0;
constexpr int OFF_E2 = 2 << 20;
constexpr int OFF_H2 = 4 << 20;
constexpr int OFF_SM = 6 << 20;
constexpr int OFF_MOD = OFF_SM + 524288;
constexpr int OFF_RK = 7 << 20;
constexpr int OFF_GU = OFF_SM + 65536;
constexpr int OFF_CK = OFF_SM + 262144;
constexpr int OFF_CZ = (7 << 20) + 524288;
constexpr int OFF_EP = OFF_SM + 131072;
constexpr int OFF_FL3 = OFF_SM + 196608;
constexpr int OFF_ST = 8 << 20;
constexpr int WS_BYTES = 16 << 20;
constexpr int OOB = 0x7ffffff0;

constexpr int KC_STRIDE = 4160;
constexpr int L_KC = 0;
constexpr int L_U = 33280;
constexpr int L_Z = L_U + 9216;
constexpr int L_W = L_Z + 8320;
constexpr int L_KV = L_W + 57344;
constexpr int L_KD = L_KV;
constexpr int L_SC = L_KV + 18432;
constexpr int L_MOD = L_SC + 1024;
constexpr int L_MP = L_MOD + 12288 + 32;
constexpr int L_GM = L_MP + 512;
constexpr int L_CNT = L_GM + 64;
constexpr int L_RANK = L_CNT + 64;
constexpr int L_SEL = L_RANK + 64;
constexpr int L_LIST = L_SEL + 32;
constexpr int L_END = L_LIST + 4096;

#define STAMP(i) do { } while (0)
#define FLUSH() do { } while (0)

template <int N> struct Int { static constexpr int value = N; };
#define PIN(x) asm volatile("" : "+v"(x))
#define PINS(x) asm volatile("" : "+s"(x))
#define PIN8(a) asm volatile("" : "+v"((a)[0]), "+v"((a)[1]), "+v"((a)[2]), "+v"((a)[3]), "+v"((a)[4]), "+v"((a)[5]), "+v"((a)[6]), "+v"((a)[7]))
#define FENCE() asm volatile("" ::: "memory")
#define RETRY(bad) if (__builtin_expect(bad, 0)) _Pragma("unroll 1") for (int spins = 0; spins < SPIN_CAP && (spins == 0 || (bad)); ++spins)
#define SCHED() __builtin_amdgcn_sched_barrier(0)
__device__ __forceinline__ float lo2f(u32 w) { return __uint_as_float(w << 16); }
__device__ __forceinline__ float hi2f(u32 w) { return __uint_as_float(w & 0xffff0000u); }
__device__ __forceinline__ f32x2 up2(u32 w) { return (f32x2){lo2f(w), hi2f(w)}; }
__device__ __forceinline__ u32 pk2(f32x2 v) { return __builtin_bit_cast(u32, __builtin_convertvector(v, bf16x2)); }
__device__ __forceinline__ u32 pk(float lo, float hi) { return pk2((f32x2){lo, hi}); }
__device__ __forceinline__ u32 nos(u32 x) { return min(x, SENT - 1u); }
__device__ __forceinline__ float rb(float x) { return (float)(__bf16)x; }
__device__ __forceinline__ f32x2 rb2(f32x2 v) { return up2(pk2(v)); }
__device__ __forceinline__ float mul1(float a, float b) { float r; asm("s_nop 0\n\tv_mul_f32 %0, %1, %2" : "=v"(r) : "v"(a), "v"(b)); return r; }
__device__ __forceinline__ float add1(float a, float b) { float r; asm("s_nop 0\n\tv_add_f32 %0, %1, %2" : "=v"(r) : "v"(a), "v"(b)); return r; }
__device__ __forceinline__ float mul0(float a, float b) { float r; asm("v_mul_f32 %0, %1, %2" : "=v"(r) : "v"(a), "v"(b)); return r; }
__device__ __forceinline__ float add0(float a, float b) { float r; asm("v_add_f32 %0, %1, %2" : "=v"(r) : "v"(a), "v"(b)); return r; }
__device__ __forceinline__ float fma0(float a, float b, float c) { float r; asm("v_fma_f32 %0, %1, %2, %3" : "=v"(r) : "v"(a), "v"(b), "v"(c)); return r; }
__device__ __forceinline__ u32 fold2(u32 a, u32 b) { return a + __builtin_amdgcn_alignbit(b, b, 21); }
__device__ __forceinline__ u32 umax4(u32x4 x) { return max(max(x.x, x.y), max(x.z, x.w)); }
__device__ __forceinline__ void bar() { asm volatile("s_waitcnt lgkmcnt(0)\n\ts_barrier" ::: "memory"); }
__device__ __forceinline__ void drain() { asm volatile("s_waitcnt vmcnt(0)" ::: "memory"); }
__device__ __forceinline__ rsrc_t mkr(const void* p, int bytes) { return __builtin_amdgcn_make_buffer_rsrc((void*)p, 0, bytes, 0x00020000); }
__device__ __forceinline__ u32x4 ld128(rsrc_t r, int off, int aux) {
  return aux == SC1 ? __builtin_amdgcn_raw_buffer_load_b128(r, off, 0, SC1)
                    : (aux == NT ? __builtin_amdgcn_raw_buffer_load_b128(r, off, 0, NT) : __builtin_amdgcn_raw_buffer_load_b128(r, off, 0, 0));
}
__device__ __forceinline__ void st128(u32x4 v, rsrc_t r, int off) { __builtin_amdgcn_raw_buffer_store_b128(v, r, off, 0, SC1); }
__device__ __forceinline__ f32x4 mfma(u32x4 a, u32x4 b, f32x4 c) {
  return __builtin_amdgcn_mfma_f32_16x16x32_bf16(__builtin_bit_cast(bf16x8, a), __builtin_bit_cast(bf16x8, b), c, 0, 0, 0);
}
__device__ __forceinline__ void dma16(rsrc_t r, int voff, int soff, u32 ldsaddr) {
  asm volatile("s_mov_b32 m0, %2\n\ts_nop 0\n\tbuffer_load_dwordx4 %0, %1, %3 offen nt lds" :: "v"(voff), "s"(r), "s"(ldsaddr), "s"(soff) : "memory");
}
__device__ __forceinline__ void dmaw(rsrc_t r, int voff, int soff, u32 ldsaddr) {
  if (WNT) dma16(r, voff, soff, ldsaddr);
  else asm volatile("s_mov_b32 m0, %2\n\ts_nop 0\n\tbuffer_load_dwordx4 %0, %1, %3 offen lds" :: "v"(voff), "s"(r), "s"(ldsaddr), "s"(soff) : "memory");
}
__device__ __forceinline__ u32x2 trread(const char* p) {
  return __builtin_bit_cast(u32x2, __builtin_amdgcn_ds_read_tr16_b64_v4i16((LAS s16x4*)(LAS char*)p));
}
__device__ __forceinline__ float dot2(u32 a, u32 b, float c) {
  return __builtin_amdgcn_fdot2_f32_bf16(__builtin_bit_cast(bf16x2, a), __builtin_bit_cast(bf16x2, b), c, false);
}
__device__ __forceinline__ void dma16_sc1(rsrc_t r, int voff, int soff, u32 ldsaddr) {
  asm volatile("s_mov_b32 m0, %2\n\ts_nop 0\n\tbuffer_load_dwordx4 %0, %1, %3 offen sc1 lds" :: "v"(voff), "s"(r), "s"(ldsaddr), "s"(soff) : "memory");
}
__device__ __forceinline__ u32x4 trfrag(const char* p, int q4) {
  const u32x2 a = __builtin_bit_cast(u32x2, __builtin_amdgcn_ds_read_tr16_b64_v4i16((LAS s16x4*)(LAS char*)p));
  const u32x2 c = __builtin_bit_cast(u32x2, __builtin_amdgcn_ds_read_tr16_b64_v4i16((LAS s16x4*)(LAS char*)(p + q4)));
  return (u32x4){a.x, a.y, c.x, c.y};
}
template <int CTRL> __device__ __forceinline__ u32 dppu(u32 x) { return (u32)__builtin_amdgcn_mov_dpp((int)x, CTRL, 0xf, 0xf, true); }
template <int CTRL> __device__ __forceinline__ float dppf(float x) { return __uint_as_float(dppu<CTRL>(__float_as_uint(x))); }
constexpr int DPP_X1 = 0xB1, DPP_X2 = 0x4E, DPP_HM = 0x141, DPP_M = 0x140;
__device__ __forceinline__ float half_sum(float v) {
  v += dppf<DPP_X1>(v); v += dppf<DPP_X2>(v); v += dppf<DPP_HM>(v); v += dppf<DPP_M>(v);
  auto s = __builtin_amdgcn_permlane16_swap(__float_as_uint(v), __float_as_uint(v), false, false);
  return __uint_as_float(s[0]) + __uint_as_float(s[1]);
}
__device__ __forceinline__ void unpack16(u32x4 a, u32x4 c, f32x2* o) {
#pragma unroll
  for (int e = 0; e < 4; ++e) { o[e] = up2(a[e]); o[4 + e] = up2(c[e]); }
}
__device__ __forceinline__ u32 rank16(const char* lds, int lane, u32& mine) {
  mine = *(const u32*)(lds + (lane & 15) * 4);
  const u32x4 o = *(const u32x4*)(lds + (lane >> 4) * 16);
  u32 c = (o.x > mine) + (o.y > mine) + (o.z > mine) + (o.w > mine);
  auto s = __builtin_amdgcn_permlane16_swap(c, c, false, false);
  c = s[0] + s[1];
  auto t = __builtin_amdgcn_permlane32_swap(c, c, false, false);
  return t[0] + t[1];
}
__device__ __forceinline__ u32 arrive(u32 cnt, int idx) {
  u32 old, tmp;
  asm volatile("s_waitcnt lgkmcnt(0)\n\ts_mov_b64 exec, 1\n\tv_mov_b32 %1, 1\n\tds_add_rtn_u32 %1, %2, %1 offset:%3\n\ts_mov_b64 exec, -1\n\t"
               "s_waitcnt lgkmcnt(0)\n\tv_readfirstlane_b32 %0, %1" : "=s"(old), "=&v"(tmp) : "v"(cnt), "i"(idx * 4) : "memory");
  return old;
}
__device__ __forceinline__ void signal(u32 cnt, int idx) {
  u32 tmp;
  asm volatile("s_waitcnt lgkmcnt(0)\n\ts_mov_b64 exec, 1\n\tv_mov_b32 %0, 1\n\tds_add_u32 %1, %0 offset:%2\n\ts_mov_b64 exec, -1"
               : "=&v"(tmp) : "v"(cnt), "i"(idx * 4) : "memory");
}
__device__ __forceinline__ u32 peek(u32 cnt, int idx) {
  u32 v, tmp;
  asm volatile("ds_read_b32 %1, %2 offset:%3\n\ts_waitcnt lgkmcnt(0)\n\tv_readfirstlane_b32 %0, %1" : "=s"(v), "=&v"(tmp) : "v"(cnt), "i"(idx * 4) : "memory");
  return v;
}
template <int SLEEP> __device__ __forceinline__ void await(u32 cnt, int idx, u32 n) {
#pragma unroll 1
  for (int spins = 0; peek(cnt, idx) < n && spins < (SPIN_CAP << 6); ++spins) if (SLEEP) __builtin_amdgcn_s_sleep(SLEEP);
}

typedef u32 u32x16 __attribute__((ext_vector_type(16)));
typedef u64 u64x8 __attribute__((ext_vector_type(8)));
typedef u64 u64x2 __attribute__((ext_vector_type(2)));
constexpr u64 desc_hi(u32 bytes) { return (u64)bytes | ((u64)0x00020000u << 32); }
#define SUB4(v, i) __builtin_shufflevector(v, v, 4 * (i), 4 * (i) + 1, 4 * (i) + 2, 4 * (i) + 3)

__global__ __launch_bounds__(512, 2) void fused_kernel(
    char* a0, u64 a1, const u16* a2, u64 a3, const u16* a4, u64 a5, const u16* a6, u64 a7, const u16* a8, const u16* a9,
    const u16* a10, const u16* a11, const u16* a12, const u16* a13, const u16* a14, const u16* a15, const u16* a16, u16* a17) {
  __shared__ __attribute__((aligned(16))) char lds[L_END];
  u32x16 ka0; u64x8 ka1; u64x2 ka2;
  asm volatile("s_load_dwordx16 %0, %3, 0x0\n\ts_load_dwordx16 %1, %3, 0x40\n\ts_load_dwordx4 %2, %3, 0x80"
               : "=&s"(ka0), "=&s"(ka1), "=&s"(ka2) : "s"(__builtin_amdgcn_kernarg_segment_ptr()));
  SCHED();
  STAMP(0);
  const int tid = threadIdx.x, lane = tid & 63;
  const int w = __builtin_amdgcn_readfirstlane(tid >> 6);
  const int b = blockIdx.x, g = b & 7, m = b >> 3, j = 32 * g + m, cg = (g + 1) & 7;
  u32x4 s4 = {SENT, SENT, SENT, SENT};

  u32 cwm = 0;
  if (CWARM && w == 7) {
    u64 pc;
    asm volatile("s_getpc_b64 %0" : "=s"(pc));
    const u32 co = (lane * 32 + m) * 128;
    if (co < CODE_BYTES) cwm = *(const __attribute__((address_space(1))) u32*)(pc + co);
    FENCE();
  }
  const u32 ldsb = (u32)(size_t)(LAS char*)lds;
  int kvo = ((tid >> 3) << 10) | ((((tid & 7) ^ ((tid >> 3) & 2) ^ ((tid >> 4) & 4)) ^ 4) << 4);
  u32 cls = OFF_E1 + cg * 262144 + m * 256, kvs = m * 65536 + cg * 128, kml = ldsb + L_KV + 1024 + w * 1024, vml = kml + 8192;
  int kho = (lane >= 40 && m > 0) ? ((kvo & 0x1fff) ^ 64) : OOB;
  int sko = lane < 32 ? lane * 16 : OOB;
  asm volatile("" : "+v"(kvo), "+v"(kho), "+v"(sko));
  asm volatile("" : "+s"(kvs), "+s"(kml), "+s"(vml));
  SCHED();
  asm volatile("s_waitcnt lgkmcnt(0)" : "+s"(ka0), "+s"(ka1), "+s"(ka2));
  asm volatile("s_mov_b32 m0, %3\n\ts_nop 0\n\tbuffer_load_dwordx4 %0, %1, %2 offen nt lds\n\t"
               "s_mov_b32 m0, %5\n\ts_nop 0\n\tbuffer_load_dwordx4 %0, %4, %2 offen nt lds"
               :: "v"(kvo), "s"(SUB4(ka0, 1)), "s"(kvs), "s"(kml), "s"(SUB4(ka0, 2)), "s"(vml) : "memory");
  if (w == 4)
    asm volatile("s_mov_b32 m0, %3\n\ts_nop 0\n\tbuffer_load_dwordx4 %0, %1, %2 offen nt lds"
                 :: "v"(kho), "s"(SUB4(ka0, 1)), "s"(m > 0 ? kvs - 8192 : 0u), "s"(ldsb + L_KV) : "memory");
  if (w == 5)
    asm volatile("s_mov_b32 m0, %3\n\ts_nop 0\n\tbuffer_load_dwordx4 %0, %1, %2 offen lds"
                 :: "v"(sko), "s"(SUB4(ka0, 3)), "s"(cg * 512), "s"(ldsb + L_KV + 17408) : "memory");
  SCHED();
  char* ws = (char*)(((u64)ka0[1] << 32) | ka0[0]);
  const u16 *gssmk = (const u16*)ka1[0], *gc = (const u16*)ka1[1], *gwmod = (const u16*)ka1[2], *gq = (const u16*)ka1[3],
            *gssmD = (const u16*)ka1[4], *gwr = (const u16*)ka1[5], *gwg = (const u16*)ka1[6], *gwu = (const u16*)ka1[7], *gwd = (const u16*)ka2[0];
  u16* gout = (u16*)ka2[1];
  const rsrc_t rW = mkr(ws, WS_BYTES);
  const bool jobG = g == 7, jobF = g < 7 && m >= 16, jobM = g < 6 && m < 16;
  const int mcol = g * 512 + m * 32;
  int e1_out = cls + ((w & 3) * 8 + ((lane & 15) >> 1)) * 8192 + (w >> 2) * 128 + (lane & 1) * 64 + (lane >> 4) * 16;
  STAMP(16);
  if (w >= 6) {
    const int at = tid - 384;
    int zo = L_CNT + (at & 3) * 16;
    if (at < 60) zo = L_U + (at >= 30 ? 4608 - 480 : 0) + at * 16;
    if (at >= 64 && at < 80) zo = L_KC + ((at >> 1) & 7) * KC_STRIDE + (at & 1) * 16;
    if (at >= 80 && at < 84) zo = L_LIST + (at & 3) * 16;
    *(u32x4*)(lds + zo) = (u32x4){0, 0, 0, 0};
  }
  const int f0 = g * 256 + (m - 16) * 16;
  const int gd0 = m * 16;
  u32 cnt = ldsb + L_CNT;
  PIN(cnt);
  u64 ep;
#define EPOCH() asm volatile("s_load_dwordx2 %0, %1, %2\n\ts_waitcnt lgkmcnt(0)" : "=&s"(ep) : "s"(ws), "s"(OFF_EP) : "memory"); STAMP(18)
  u32x2 tagv;
#define MAKE_TAG() do { const u64 tag = ep + 1ull; tagv = (u32x2){(u32)tag, (u32)(tag >> 32)}; } while (0)
  u32x2 kx;
  u32 cw, dpair, pf0;
  if (w < 4) {
    const rsrc_t rS = mkr(gssmk, S * D * 2);
    const u32 dst = ldsb + L_KC + (w >> 1) * (4 * KC_STRIDE) + 32 + (w & 1) * 2048;
    dma16(rS, lane * 16, j * 8192 + w * 2048, dst);
    dma16(rS, lane * 16, j * 8192 + w * 2048 + 1024, dst + 1024);
  }
  {
    const int at = tid - 256;
    const u16* p0 = w < 4 ? gssmk : gwr;
    const u16* p1 = jobG ? gwd : gwmod;
    PINS(p0); PINS(p1);
    kx = __builtin_amdgcn_raw_buffer_load_b64(mkr(p0, S * D * 2), w < 4 ? j * 8192 + w * 2048 + 2048 : (at & 127) * 8, 0, 0);
    const int pf_g = m * 57344 + at * 128, pf_m = (m * 16 + (at >> 3)) * 6144 + g * 1024 + (at & 7) * 128;
    const u32 pf_n = jobG ? 192 : (g < 6 ? 128 : 0);
    pf0 = __builtin_amdgcn_raw_buffer_load_b32(mkr(p1, D * 6 * D * 2), (u32)at < pf_n ? (jobG ? pf_g : pf_m) : OOB, 0, PFAUX);
    FENCE();
    if (w >= 4) {
      cw = __builtin_amdgcn_raw_buffer_load_b32(mkr(gc, D * 2), jobM ? at * 4 : OOB, 0, 0);
      dpair = __builtin_amdgcn_raw_buffer_load_b32(mkr(gssmD, D * 2), j * 4, 0, 0);
    }
    FENCE();
  }
  EPOCH();
  MAKE_TAG();
  {
    const int blk = 4 * (w >> 2) + (lane >> 4) + 1;
    int ko = (blk * 8 + ((lane & 15) >> 2)) * 128 + (((w & 3) ^ ((lane >> 3) & 1) ^ ((blk & 1) << 1)) << 5) + (lane & 3) * 8;
    int cko = (lane & 17) == 0 ? OFF_CK + ((cg * 4 + (w & 3)) * 64 + m * 2 + (w >> 2)) * 64 + (lane & 14) * 4 + (lane >> 5) * 4 : OOB;
    u32 tw = lane < 32 ? tagv.x : tagv.y;
    PIN(ko); PIN(e1_out); PIN(cko); PIN(tw);
    int sko2 = L_KV + 17408 + ((w & 3) * 16 + (lane & 15)) * 8, ko2 = L_KV - 512 + (ko ^ 64);
    PIN(sko2); PIN(ko2);
    asm volatile("s_waitcnt vmcnt(4)\n\ts_barrier" ::: "memory");
    STAMP(20);
    const char* kp = lds + L_KV + ko;
    const u32x2 sk = *(const u32x2*)(lds + sko2);
    const u32x2 p0 = trread(lds + ko2), p1 = trread(kp), p2 = trread(kp + 512), v0 = trread(kp + 8192), v1 = trread(kp + 8704);
    const u32 c10 = __builtin_amdgcn_alignbyte(sk.x, sk.x, 2), c32 = __builtin_amdgcn_alignbyte(sk.y, sk.y, 2);
    const u32 c0z = sk.x & 0xffffu, cz3 = sk.y & 0xffff0000u, c21 = (sk.y & 0xffffu) | (sk.x & 0xffff0000u);
    const u32 P[6] = {p0.x, p0.y, p1.x, p1.y, p2.x, p2.y}, V[4] = {v0.x, v0.y, v1.x, v1.y};
    u32 o[4];
#pragma unroll
    for (int i = 0; i < 4; ++i) {
      const float ev = dot2(P[i + 2], c0z, dot2(P[i + 1], c21, dot2(P[i], cz3, 0.f)));
      const float od = dot2(P[i + 2], c10, dot2(P[i + 1], c32, 0.f));
      o[i] = pk(od * hi2f(V[i]), ev * lo2f(V[i]));
    }
    st128((u32x4){o[3], o[2], o[1], o[0]}, rW, e1_out);
    FENCE();
    {
      auto t = __builtin_amdgcn_permlane32_swap(fold2(o[3], o[2]), fold2(o[1], o[0]), false, false);
      u32 hs = t[0] + t[1];
      auto u = __builtin_amdgcn_permlane16_swap(hs, hs, false, false);
      hs = u[0] + u[1];
      hs += dppu<DPP_X1>(hs);
      __builtin_amdgcn_raw_buffer_store_b32(hs + tw, rW, cko, 0, SC1);
    }
    FENCE();
    STAMP(2);
  }
  const rsrc_t rO = mkr(gout, S * D * 2);

  if (w < 4) {
    const int ch = w >> 1, half = w & 1, r = lane & 15, h = lane >> 4;
    if (MPOS == 0 && jobM) signal(cnt, 12);
    int cki = OFF_CK + ((g * 4 + (m >> 3)) * 64 + w * 8 + (lane >> 3)) * 64 + (m & 7) * 8, e1i = OFF_E1 + j * 8192 + tid * 16;
    int o_z = OFF_E2 + j * 8192 + (128 * w + 4 * r + h) * 16;
    int o_cz = OFF_CZ + j * 2048 + w * 512 + r * 16 + h * 4;
    u32 tw2 = (h & 1) ? tagv.y : tagv.x;
    PIN(cki); PIN(e1i); PIN(o_z); PIN(o_cz); PIN(tw2);
    asm volatile("" : "+v"(kx), "+v"(pf0) :: "memory");
    STAMP(21);
    {
      char* kc = lds + L_KC + ch * (4 * KC_STRIDE) + (half * 128 + lane + 2) * 16;
      u32x4 kd[2];
      u32x2 kn[2];
#pragma unroll
      for (int i = 0; i < 2; ++i) { kd[i] = *(const u32x4*)(kc + i * 1024); kn[i] = *(const u32x2*)(kc + i * 1024 + 16); }
      if (lane == 63) kn[1] = kx;
#pragma unroll
      for (int i = 0; i < 2; ++i) {
        const u32 a01 = __builtin_amdgcn_alignbyte(kd[i].y, kd[i].x, 2), a12 = __builtin_amdgcn_alignbyte(kd[i].z, kd[i].y, 2);
        const u32 a23 = __builtin_amdgcn_alignbyte(kd[i].w, kd[i].z, 2), a34 = __builtin_amdgcn_alignbyte(kn[i].x, kd[i].w, 2);
        const u32 a45 = __builtin_amdgcn_alignbyte(kn[i].y, kn[i].x, 2);
        *(u32x4*)(kc + i * 1024 + KC_STRIDE) = (u32x4){a01, a12, a23, a34};
        *(u32x4*)(kc + i * 1024 + 2 * KC_STRIDE) = (u32x4){kd[i].y, kd[i].z, kd[i].w, kn[i].x};
        *(u32x4*)(kc + i * 1024 + 3 * KC_STRIDE) = (u32x4){a12, a23, a34, a45};
        if (i == 0 && half == 0 && lane == 0) {
          *(u32*)(kc - 4 + KC_STRIDE) = kd[0].x << 16;
          *(u32*)(kc - 4 + 2 * KC_STRIDE) = kd[0].x;
          *(u32x2*)(kc - 8 + 3 * KC_STRIDE) = (u32x2){kd[0].x << 16, a01};
        }
      }
    }
    STAMP(22);
    if (ch == 0) { if (arrive(cnt, 6) != 1) await<0>(cnt, 6, 2); } else { if (arrive(cnt, 7) != 1) await<0>(cnt, 7, 2); }
    STAMP(3);
    if (E1SLP < 0) drain(); else if (E1SLP) __builtin_amdgcn_s_sleep(E1SLP);
    STAMP(4);
    u32x4 x0 = ld128(rW, e1i, SC1), x1 = ld128(rW, e1i + 4096, SC1);
    u32x2 c0 = __builtin_amdgcn_raw_buffer_load_b64(rW, cki, 0, SC1), c1 = __builtin_amdgcn_raw_buffer_load_b64(rW, cki + 2048, 0, SC1);
    FENCE();
    if (MPOS == 2 && jobM) signal(cnt, 12);
    u32x2 Al[32], Ah[32];
    {
      const u32 ka = ldsb + L_KC + ch * (4 * KC_STRIDE) + ((1 + r) & 3) * KC_STRIDE + 2 * (128 * half + 8 * h + ((1 + r) & ~3));
#define RD4(i) asm volatile("ds_read_b64 %0, %8 offset:%9\n\tds_read_b64 %1, %8 offset:%9+8\n\tds_read_b64 %2, %8 offset:%9+64\n\t" \
      "ds_read_b64 %3, %8 offset:%9+72\n\tds_read_b64 %4, %8 offset:%9+128\n\tds_read_b64 %5, %8 offset:%9+136\n\t" \
      "ds_read_b64 %6, %8 offset:%9+192\n\tds_read_b64 %7, %8 offset:%9+200" \
      : "=&v"(Al[i]), "=&v"(Ah[i]), "=&v"(Al[8 + i]), "=&v"(Ah[8 + i]), "=&v"(Al[16 + i]), "=&v"(Ah[16 + i]), "=&v"(Al[24 + i]), "=&v"(Ah[24 + i]) \
      : "v"(ka), "i"(512 * i) : "memory")
      RD4(0); RD4(1); RD4(2); RD4(3); RD4(4); RD4(5); RD4(6); RD4(7);
      asm volatile("s_waitcnt lgkmcnt(0)" ::: "memory");
      PIN8(Al); PIN8(Al + 8); PIN8(Al + 16); PIN8(Al + 24); PIN8(Ah); PIN8(Ah + 8); PIN8(Ah + 16); PIN8(Ah + 24);
    }
    STAMP(5);
    {
      auto bad = [&]() __attribute__((always_inline)) {
        u32 h[4] = {fold2(x0.x, x0.y), fold2(x0.z, x0.w), fold2(x1.x, x1.y), fold2(x1.z, x1.w)};
#pragma unroll
        for (int i = 0; i < 4; ++i) h[i] += dppu<DPP_X1>(h[i]);
#pragma unroll
        for (int i = 0; i < 4; ++i) h[i] += dppu<DPP_X2>(h[i]);
#pragma unroll
        for (int i = 0; i < 4; ++i) h[i] += dppu<DPP_HM>(h[i]);
        return (((h[0] + tagv.x) ^ c0.x) | ((h[1] + tagv.y) ^ c0.y) | ((h[2] + tagv.x) ^ c1.x) | ((h[3] + tagv.y) ^ c1.y)) != 0;
      };
      bool nb = bad();
      RETRY(__any(nb)) {
        FENCE();
        if (nb) {
          x0 = ld128(rW, e1i, SC1); x1 = ld128(rW, e1i + 4096, SC1);
          c0 = __builtin_amdgcn_raw_buffer_load_b64(rW, cki, 0, SC1); c1 = __builtin_amdgcn_raw_buffer_load_b64(rW, cki + 2048, 0, SC1);
        }
        asm volatile("" : "+v"(x0), "+v"(x1), "+v"(c0), "+v"(c1));
        nb = bad();
      }
      STAMP(1);
      char* u0 = lds + L_U + ((tid >> 2) & 1) * 4608 + (30 + (tid >> 3) * 4 + (tid & 3)) * 16;
      *(u32x4*)(u0) = x0;
      *(u32x4*)(u0 + 2048) = x1;
    }
    if (arrive(cnt, 3) != 3) await<0>(cnt, 3, 4);
    STAMP(6);
    {
      f32x4 acc[8];
#pragma unroll
      for (int t = 0; t < 8; ++t) acc[t] = (f32x4){0, 0, 0, 0};
      const char* ub = lds + L_U + ch * 4608 + (30 + 2 * r + 1 - 16 * half - h) * 16;
      char* rp = lds + L_KC + w * 8192 + lane * 16;
      u32x4 B0[8], B1[8];
#pragma unroll
      for (int t = 0; t < 8; ++t) B0[t] = *(const u32x4*)(ub + 512 * t);
#pragma unroll
      for (int t = 0; t < 8; ++t) B1[t] = *(const u32x4*)(ub + 512 * t - 64);
      SCHED();
#define AF(ww, p) (u32x4){Al[8 * (ww) + (p)].x, Al[8 * (ww) + (p)].y, Ah[8 * (ww) + (p)].x, Ah[8 * (ww) + (p)].y}
#pragma unroll
      for (int bp = 0; bp < 8; ++bp) {
#pragma unroll
        for (int t = 7; t >= bp; --t) acc[t] = mfma(AF(0, t - bp), B0[bp], acc[t]);
        SCHED();
      }
#pragma unroll
      for (int bp = 0; bp < 8; ++bp) {
        B0[bp] = *(const u32x4*)(ub + 512 * bp - 128);
#pragma unroll
        for (int t = 7; t >= bp; --t) acc[t] = mfma(AF(1, t - bp), B1[bp], acc[t]);
        SCHED();
      }
#pragma unroll
      for (int bp = 0; bp < 8; ++bp) {
        B1[bp] = *(const u32x4*)(ub + 512 * bp - 192);
#pragma unroll
        for (int t = 7; t >= bp; --t) acc[t] = mfma(AF(2, t - bp), B0[bp], acc[t]);
        SCHED();
      }
#pragma unroll
      for (int t = 0; t < 8; ++t) {
#pragma unroll
        for (int bp = 0; bp <= t; ++bp) {
          acc[t] = mfma(AF(3, t - bp), B1[bp], acc[t]);
          if (t >= 2 && bp == 1) { SCHED(); *(f32x4*)(rp + (t - 1) * 1024) = acc[t - 1]; if (t == 2) *(f32x4*)(rp) = acc[0]; SCHED(); }
        }
        SCHED();
      }
      *(f32x4*)(rp + 7 * 1024) = acc[7];
    }
    STAMP(7);
    if (arrive(cnt, 14) != 7) await<0>(cnt, 14, 8);
    u32x4 zt[2];
#pragma unroll
    for (int t = 0; t < 2; ++t) {
      const char* rp = lds + L_KC + (2 * w + t) * 1024 + lane * 16;
      const char* dp = lds + L_KD + (2 * w + t) * 1024 + lane * 16;
      const f32x4 ya = *(const f32x4*)(rp) + *(const f32x4*)(rp + 8192) + *(const f32x4*)(dp);
      const f32x4 yb = *(const f32x4*)(rp + 16384) + *(const f32x4*)(rp + 24576) + *(const f32x4*)(dp + 8192);
      zt[t] = (u32x4){pk(ya[0], yb[0]), pk(ya[1], yb[1]), pk(ya[2], yb[2]), pk(ya[3], yb[3])};
      st128(zt[t], rW, o_z + 1024 * t);
    }
    FENCE();
#pragma unroll
    for (int t = 0; t < 2; ++t) {
      auto u = __builtin_amdgcn_permlane16_swap(fold2(zt[t].x, zt[t].y), fold2(zt[t].z, zt[t].w), false, false);
      __builtin_amdgcn_raw_buffer_store_b32(u[0] + u[1] + tw2, rW, o_cz + 256 * t, 0, SC1);
    }
    signal(cnt, 9);
    STAMP(15);
    u32x4 Wf[14];
    const int wvo = jobG ? (w * 448 + (lane >> 1)) * 1024 + gd0 * 2 + (lane & 1) * 16 : (w * 128 + (lane >> 1)) * (F * 2) + f0 * 2 + (lane & 1) * 16;
    if (F1POS == 0 && jobF) {
#pragma unroll
      for (int i = 0; i < 4; ++i) dmaw(mkr(gwg, D * F * 2), wvo, i * 32 * F * 2, ldsb + L_W + w * 4096 + i * 1024);
    } else if (jobG) {
#pragma unroll
      for (int i = 0; i < 4; ++i) dmaw(mkr(gwd, F * D * 2), wvo, i * 32 * 1024, ldsb + L_W + w * 14336 + i * 1024);
    }
    __builtin_amdgcn_s_sleep(ZPACE);
    STAMP(8);
    {
      const int zo = OFF_E2 + (tid >> 1) * 8192 + j * 32 + (tid & 1) * 16, zc = OFF_CZ + (tid >> 1) * 2048 + j * 8 + (tid & 1) * 4;
      u32x4 z0 = ld128(rW, zo, SC1), z1 = ld128(rW, zo + 128 * 8192, SC1);
      u32 k0 = __builtin_amdgcn_raw_buffer_load_b32(rW, zc, 0, SC1), k1 = __builtin_amdgcn_raw_buffer_load_b32(rW, zc + 128 * 2048, 0, SC1);
      const u32 tw3 = (lane & 1) ? tagv.y : tagv.x;
      auto bad = [&]() __attribute__((always_inline)) {
        const u32 a0 = fold2(z0.x, z0.y), a1 = fold2(z0.z, z0.w), b0 = fold2(z1.x, z1.y), b1 = fold2(z1.z, z1.w);
        const u32 sa0 = a0 + dppu<DPP_X1>(a0), sa1 = a1 + dppu<DPP_X1>(a1), sb0 = b0 + dppu<DPP_X1>(b0), sb1 = b1 + dppu<DPP_X1>(b1);
        u32 d = ((((lane & 1) ? sa1 : sa0) + tw3) ^ k0) | ((((lane & 1) ? sb1 : sb0) + tw3) ^ k1);
        return d;
      };
      u32 nb = bad();
      STAMP(17);
      RETRY(__any(nb != 0)) {
        FENCE();
        nb |= dppu<DPP_X1>(nb);
        if (nb) {
          z0 = ld128(rW, zo, SC1); z1 = ld128(rW, zo + 128 * 8192, SC1);
          k0 = __builtin_amdgcn_raw_buffer_load_b32(rW, zc, 0, SC1); k1 = __builtin_amdgcn_raw_buffer_load_b32(rW, zc + 128 * 2048, 0, SC1);
        }
        asm volatile("" : "+v"(z0), "+v"(z1), "+v"(k0), "+v"(k1));
        nb = bad();
      }
      STAMP(9);
      char* zp = lds + L_Z + (tid & 1) * 4160 + (tid >> 1) * 4;
#pragma unroll
      for (int e = 0; e < 4; ++e) { *(u32*)(zp + e * 1040) = z0[e]; *(u32*)(zp + e * 1040 + 512) = z1[e]; }
    }
    bar();
    if (!(jobF || jobG)) { FLUSH(); return; }
    await<2>(cnt, 10, 4);
    if (jobG) {
#pragma unroll
      for (int i = 4; i < 8; ++i) dmaw(mkr(gwd, F * D * 2), wvo, i * 32 * 1024, ldsb + L_W + w * 14336 + i * 1024);
    } else {
#pragma unroll
      for (int i = 0; i < 4; ++i) dmaw(mkr(gwu, D * F * 2), wvo, i * 32 * F * 2, ldsb + L_W + 16384 + w * 4096 + i * 1024);
    }
    __builtin_amdgcn_s_sleep(KPACE);
    STAMP(11);
    const u64 tb6 = __builtin_amdgcn_s_memrealtime();
    const int ko = OFF_RK + g * 256 * RKS + (tid >> 1) * RKS + (tid & 1) * 16;
    u32x4 ka = ld128(rW, ko, SC1), kb = ld128(rW, ko + 128 * RKS, SC1);
    if (jobF) {
      asm volatile("s_waitcnt vmcnt(2)" ::: "memory");
      const char* wp = lds + L_W + w * 4096 + h * 256 + r * 8;
#pragma unroll
      for (int i = 0; i < 8; ++i) Wf[i] = trfrag(wp + (i >> 2) * 16384 + (i & 3) * 1024, 128);
      PIN8(Wf);
    }
    asm volatile("" : "+v"(ka), "+v"(kb) :: "memory");
    u32 mx = max(umax4(ka), umax4(kb));
    RETRY(!__all(mx != SENT)) {
      FENCE();
      if (mx == SENT) { ka = ld128(rW, ko, SC1); kb = ld128(rW, ko + 128 * RKS, SC1); mx = max(umax4(ka), umax4(kb)); }
    }
    if (jobG) {
#pragma unroll
      for (int i = 8; i < 14; ++i) dmaw(mkr(gwd, F * D * 2), wvo, i * 32 * 1024, ldsb + L_W + w * 14336 + i * 1024);
    }
    STAMP(12);
    u32 gm = max(mx, dppu<DPP_X1>(mx));
    gm = max(gm, dppu<DPP_X2>(gm)); gm = max(gm, dppu<DPP_HM>(gm)); gm = max(gm, dppu<DPP_M>(gm));
    if (r == 0) *(u32*)(lds + L_GM + (4 * w + h) * 4) = gm;
    const u32 ky[8] = {ka.x, ka.y, ka.z, ka.w, kb.x, kb.y, kb.z, kb.w};
    u32 m2 = 0;
#pragma unroll
    for (int e = 0; e < 8; ++e) m2 = max(m2, ky[e] == mx ? 0u : ky[e]);
    PIN(m2);
    bar();
    u32 mine;
    u32 rk = rank16(lds + L_GM, lane, mine);
    const u32 T = __builtin_amdgcn_readlane(mine, __builtin_ctzll(__ballot(rk == 7)));
    if (mx >= T) {
      const u32 pos = __hip_atomic_fetch_add((u32*)(lds + L_CNT), 1u, __ATOMIC_RELAXED, __HIP_MEMORY_SCOPE_WORKGROUP);
      *(u32*)(lds + L_LIST + pos * 4) = mx;
    }
    if (__any(m2 >= T)) {
#pragma unroll
      for (int e = 0; e < 8; ++e)
        if (ky[e] >= T && ky[e] != mx) {
          const u32 pos = __hip_atomic_fetch_add((u32*)(lds + L_CNT), 1u, __ATOMIC_RELAXED, __HIP_MEMORY_SCOPE_WORKGROUP);
          *(u32*)(lds + L_LIST + pos * 4) = ky[e];
        }
    }
    bar();
    u32 cand = *(const u32*)(lds + L_LIST + r * 4);
    const u32 N = peek(cnt, 0);
    if (N > 16) {
      for (u32 i = tid; i < N; i += 256) {
        const u32 kx = *(const u32*)(lds + L_LIST + i * 4);
        u32 c = 0;
#pragma unroll 1
        for (u32 q = 0; q < N; ++q) c += *(const u32*)(lds + L_LIST + q * 4) > kx;
        if (c < 8) *(u32*)(lds + L_SEL + c * 4) = kx;
      }
      if (arrive(cnt, 15) != 3) await<0>(cnt, 15, 4);
      if (tid < 16) *(u32*)(lds + L_LIST + tid * 4) = tid < 8 ? *(const u32*)(lds + L_SEL + tid * 4) : 0u;
      if (arrive(cnt, 15) != 7) await<0>(cnt, 15, 8);
      cand = *(const u32*)(lds + L_LIST + r * 4);
    }
    STAMP(13);
    if (jobF) {
      const int ho = OFF_H2 + (2047 - (int)(cand & 0x7ffu)) * 1024 + (w * 128 + 8 * h) * 2;
      u32x4 a[4];
#pragma unroll
      for (int ks = 0; ks < 4; ++ks) a[ks] = ld128(rW, ho + ks * 64, SC1);
      FENCE();
      rk = rank16(lds + L_LIST, lane, mine);
      if (w == 0 && lane < 16 && rk < 8) *(u32*)(lds + L_RANK + rk * 4) = (lane >> 2) * 256 + (lane & 3) * 4;
      RETRY(!__all(max(max(umax4(a[0]), umax4(a[1])), max(umax4(a[2]), umax4(a[3]))) != SENT)) {
        FENCE();
#pragma unroll
        for (int ks = 0; ks < 4; ++ks) a[ks] = ld128(rW, ho + ks * 64, SC1);
        PIN(a[0]); PIN(a[1]); PIN(a[2]); PIN(a[3]);
      }
      f32x4 ag = {0, 0, 0, 0}, au = {0, 0, 0, 0};
#pragma unroll
      for (int ks = 0; ks < 4; ++ks) { ag = mfma(a[ks], Wf[ks], ag); au = mfma(a[ks], Wf[4 + ks], au); }
      *(f32x4*)(lds + L_KC + w * 2048 + lane * 16) = ag;
      *(f32x4*)(lds + L_KC + w * 2048 + 1024 + lane * 16) = au;
      bar();
      STAMP(14);
      if (w < 2) {
        const char* p = lds + L_KC + *(const u32*)(lds + L_RANK + (lane >> 3) * 4) + (8 * w + (lane & 7)) * 16;
        float gv = 0, uv = 0;
#pragma unroll
        for (int i = 0; i < 4; ++i) { gv += *(const float*)(p + i * 2048); uv += *(const float*)(p + i * 2048 + 1024); }
        gv = rb(gv);
        const float v = rb(gv * __builtin_amdgcn_rcpf(1.f + __expf(-gv))) * rb(uv);
        const float vn = dppf<DPP_X1>(v);
        __builtin_amdgcn_raw_buffer_store_b32(nos(pk(v, vn)), rW, (lane & 1) ? OOB : OFF_GU + f0 * 16 + w * 128 + lane * 2, 0, SC1);
        STAMP(10);
      }
    } else {
      rk = rank16(lds + L_LIST, lane, mine);
      if (w == 0 && lane < 16 && rk < 8) *(u32*)(lds + L_SEL + rk * 4) = mine;
      bar();
      drain();
      {
        const char* wp = lds + L_W + w * 14336 + h * 256 + r * 8;
#pragma unroll
        for (int i = 0; i < 14; ++i) Wf[i] = trfrag(wp + i * 1024, 128);
        PIN8(Wf); PIN8(Wf + 6);
      }
      while (__builtin_amdgcn_s_memrealtime() < tb6 + GDELAY) __builtin_amdgcn_s_sleep(1);
      STAMP(14);
      const int go = OFF_GU + (w * 56 + 4 * (r >> 3) + h) * 128 + (r & 7) * 16;
      u32x4 a[7];
#pragma unroll 1
      for (int spins = 0; spins < SPIN_CAP; ++spins) {
        FENCE();
        u32 mm = 0;
#pragma unroll
        for (int i = 0; i < 7; ++i) a[i] = ld128(rW, go + i * 1024, SC1);
#pragma unroll
        for (int i = 0; i < 7; ++i) mm = max(mm, umax4(a[i]));
        if (__all(mm != SENT)) break;
      }
      STAMP(10);
      f32x4 a0 = {0, 0, 0, 0}, a1 = {0, 0, 0, 0};
#pragma unroll
      for (int i = 0; i < 7; ++i) { a0 = mfma(a[i], Wf[2 * i], a0); a1 = mfma(a[i], Wf[2 * i + 1], a1); }
      *(f32x4*)(lds + L_KC + w * 1024 + lane * 16) = lane < 32 ? a0 : a1;
      bar();
    }
    FLUSH();
  } else {
    const int a = w - 4, at = tid - 256, hl = lane & 31, row = 8 * j + 2 * a + (lane >> 5);
    __builtin_amdgcn_s_setprio(1);
    int o_own = j * 8192 + at * 16;
    int o_m = (a * 128 + (lane >> 2)) * 6144 + mcol * 2 + (lane & 3) * 16;
    int o_w = L_W + a * 8192 + ((lane >> 4) * 8 + ((lane & 15) >> 2)) * 64 + (lane & 3) * 8;
    u32x2 wrw = kx;
    asm volatile("" : "+v"(o_own), "+v"(o_m), "+v"(o_w), "+v"(s4), "+v"(dpair), "+v"(cw), "+v"(wrw), "+v"(cwm), "+v"(pf0) :: "memory");
    u32x4 av[4];
    auto mod_request = [&]() __attribute__((always_inline)) {
      const rsrc_t rM = mkr(gwmod, D * 6 * D * 2);
#pragma unroll
      for (int i = 0; i < 8; ++i) dmaw(rM, o_m, i * 16 * 6144, ldsb + L_W + a * 8192 + i * 1024);
    };
    auto mod_compute = [&]() __attribute__((always_inline)) {
      STAMP(21);
      u32x4 bw[8];
#pragma unroll
      for (int i = 0; i < 8; ++i) bw[i] = trfrag(lds + o_w + (i >> 1) * 2048 + (i & 1) * 32, 256);
      PIN8(bw);
      f32x4 d0 = {0, 0, 0, 0}, d1 = {0, 0, 0, 0};
#pragma unroll
      for (int ks = 0; ks < 4; ++ks) { d0 = mfma(av[ks], bw[2 * ks], d0); d1 = mfma(av[ks], bw[2 * ks + 1], d1); }
      if (lane < 16) { *(float*)(lds + L_MP + a * 128 + lane * 4) = d0[0]; *(float*)(lds + L_MP + a * 128 + 64 + lane * 4) = d1[0]; }
      const u32 nth = arrive(cnt, 2);
      {
        f32x4 pp[4];
#pragma unroll
        for (int i = 0; i < 4; ++i) pp[i] = *(const f32x4*)(lds + L_MP + i * 128 + (lane & 7) * 16);
        float sm[4];
#pragma unroll
        for (int e = 0; e < 4; ++e) sm[e] = add1(add1(pp[0][e], pp[1][e]), add1(pp[2][e], pp[3][e]));
        const u32 p0 = pk(sm[0], sm[1]), p1 = pk(sm[2], sm[3]);
        st128((u32x4){p0, p1, p0 ^ tagv.x, p1 ^ tagv.y}, rW, nth == 3 ? OFF_MOD + (lane >> 3) * 12288 + mcol * 4 + (lane & 7) * 16 : OOB);
        FENCE();
      }
      STAMP(3);
    };
    if (jobM) {
      if (MPOS == 0) { await<1>(cnt, 12, 4); mod_request(); }
      f32x2 cv = up2(cw);
      cv.x = mul1(cv.x, __builtin_amdgcn_rcpf(add1(1.f, __builtin_amdgcn_exp2f(mul1(cv.x, -1.44269504088896f)))));
      cv.y = mul1(cv.y, __builtin_amdgcn_rcpf(add1(1.f, __builtin_amdgcn_exp2f(mul1(cv.y, -1.44269504088896f)))));
      *(u32*)(lds + L_SC + at * 4) = pk2(cv);
#pragma unroll
      for (int ks = 0; ks < 4; ++ks) av[ks] = *(const u32x4*)(lds + L_SC + a * 256 + ks * 64 + (lane >> 4) * 16);
      PIN(av[0]); PIN(av[1]); PIN(av[2]); PIN(av[3]);
      if (MPOS == 2) { await<1>(cnt, 12, 4); mod_request(); }
      if (MPOS == 0 || MPOS == 2) { drain(); mod_compute(); }
    }
    int o_sm = OOB, o_q = row * 1024 + hl * 16;
    if (at < 16) o_sm = OFF_RK + (at >> 1) * 256 * RKS + j * RKS + (at & 1) * 16;
    if (jobF && at >= 16 && at < 32) o_sm = OFF_GU + f0 * 16 + (at - 16) * 16;
    int o_pf = jobG ? m * 57344 + 24576 + at * 128 : (a < 2 ? (m * 16 + (lane >> 2)) * (F * 2) + g * 512 + (lane & 3) * 128 : OOB);
    const int un = 128 * a + 4 * (lane & 15) + (lane >> 4);
    int o_u = L_U + (30 + (un >> 1)) * 16 + ((un & 1) ? 0 : 8);
    int o_mod = OFF_MOD + g * 12288 + (a >> 1) * 6144 + (at & 127) * 16;
    asm volatile("" : "+v"(o_sm), "+v"(o_q), "+v"(o_pf), "+v"(o_u), "+v"(o_mod));
    await<1>(cnt, 3, 4);
    STAMP(6);
    PIN(pf0);
    if (MPOS == 1 && jobM) { mod_request(); if (arrive(cnt, 12) != 3) await<1>(cnt, 12, 4); }
    __builtin_amdgcn_raw_buffer_store_b128(s4, rW, o_own, OFF_H2, SC1);
    __builtin_amdgcn_raw_buffer_store_b128(s4, rW, o_own, OFF_H2 + 4096, SC1);
    st128(s4, rW, o_sm);
    FENCE();
    u32x4 q0 = ld128(mkr(gq, S * D * 2), o_q, NT), q1 = ld128(mkr(gq, S * D * 2), o_q + 512, NT);
    FENCE();
    u32 pf1 = __builtin_amdgcn_raw_buffer_load_b32(mkr(jobG ? gwd : (a == 0 ? gwg : gwu), D * F * 2), o_pf, 0, PFAUX);
    FENCE();
    {
      const f32x2 dv = up2(dpair);
#pragma unroll
      for (int t = 0; t < 2; ++t) {
        const u32x2 ua = *(const u32x2*)(lds + o_u + 512 * t), ub = *(const u32x2*)(lds + o_u + 512 * t + 4608);
        char* dp = lds + L_KD + (2 * a + t) * 1024 + lane * 16;
        *(f32x4*)(dp) = (f32x4){mul0(hi2f(ua.y), dv.x), mul0(lo2f(ua.y), dv.x), mul0(hi2f(ua.x), dv.x), mul0(lo2f(ua.x), dv.x)};
        *(f32x4*)(dp + 8192) = (f32x4){mul0(hi2f(ub.y), dv.y), mul0(lo2f(ub.y), dv.y), mul0(hi2f(ub.x), dv.y), mul0(lo2f(ub.x), dv.y)};
      }
    }
    if (MPOS == 1 && jobM) {
      asm volatile("s_waitcnt vmcnt(6)" ::: "memory");
      mod_compute();
      asm volatile("s_waitcnt vmcnt(4)" ::: "memory");
    } else {
      asm volatile("s_waitcnt vmcnt(3)" ::: "memory");
    }
    signal(cnt, 14);
    STAMP(5);
    asm volatile("" : "+v"(q0), "+v"(q1) :: "memory");
    STAMP(7);
    __builtin_amdgcn_s_setprio(0);
    f32x2 x[8], gah[8];
    {
      unpack16(q0, q1, x);
      float s = 0;
#pragma unroll
      for (int e = 0; e < 8; ++e) s = add0(s, add0(x[e].x, x[e].y));
      const float mean = half_sum(s) * (1.f / 512.f);
      float qq = 0;
#pragma unroll
      for (int e = 0; e < 8; ++e) { const float d0 = add0(x[e].x, -mean), d1 = add0(x[e].y, -mean); qq = fma0(d0, d0, qq); qq = fma0(d1, d1, qq); }
      const float var = rb(half_sum(qq) * (1.f / 512.f));
      float isd = __builtin_amdgcn_rcpf(rb(__builtin_amdgcn_sqrtf(rb(var + 1e-5f))));
      float mi = -rb(mean) * isd;
      PIN(isd); PIN(mi);
#pragma unroll
      for (int e = 0; e < 8; ++e) gah[e] = (f32x2){fma0(x[e].x, isd, mi), fma0(x[e].y, isd, mi)};
      PIN8(x); PIN8(gah);
    }
    STAMP(8);
    asm volatile("" : "+v"(pf1) :: "memory");
    if (F1POS == 1 && jobF) {
#pragma unroll
      for (int i = 0; i < 4; ++i) dmaw(mkr(gwg, D * F * 2), (a * 128 + (lane >> 1)) * (F * 2) + f0 * 2 + (lane & 1) * 16, i * 32 * F * 2, ldsb + L_W + a * 4096 + i * 1024);
    }
    STAMP(4);
    if (MRD == 0) await<1>(cnt, 9, 4);
    if (MODSLP) __builtin_amdgcn_s_sleep(MODSLP);
    {
      u32x4 mu[3];
#pragma unroll
      for (int i = 0; i < 3; ++i) mu[i] = ld128(rW, o_mod + i * 2048, SC1);
      auto modbad = [&]() __attribute__((always_inline)) {
        u32 d = 0;
#pragma unroll
        for (int i = 0; i < 3; ++i) d |= (mu[i].x ^ mu[i].z ^ tagv.x) | (mu[i].y ^ mu[i].w ^ tagv.y);
        return d != 0;
      };
      bool nb = modbad();
      RETRY(__any(nb)) {
        FENCE();
        __builtin_amdgcn_s_sleep(3);
        if (nb) {
#pragma unroll
          for (int i = 0; i < 3; ++i) mu[i] = ld128(rW, o_mod + i * 2048, SC1);
        }
        asm volatile("" : "+v"(mu[0]), "+v"(mu[1]), "+v"(mu[2]));
        nb = modbad();
      }
      __builtin_amdgcn_s_setprio(1);
      STAMP(17);
      const f32x2 s0 = up2(mu[0].x), s1 = up2(mu[0].y), c0 = rb2(up2(mu[1].x) + 1.f), c1 = rb2(up2(mu[1].y) + 1.f), g0 = up2(mu[2].x), g1 = up2(mu[2].y);
      char* mo = lds + L_MOD + (at & 1) * 1024 + ((at & 127) >> 1) * 16;
      auto put = [&](int v, f32x2 lo, f32x2 hi) __attribute__((always_inline)) { *(f32x4*)(mo + v * 2048) = (f32x4){lo.x, lo.y, hi.x, hi.y}; };
      if (a < 2) {
        put(0, g0 * c0, g1 * c1);
        put(1, g0 * s0, g1 * s1);
      } else {
        const f32x2 w0 = up2(wrw.x), w1 = up2(wrw.y), r0 = c0 * w0, r1 = c1 * w1, sa_ = r0 + r1, sc_ = s0 * w0 + s1 * w1;
        put(2, r0, r1); put(3, c0, c1); put(4, s0, s1); put(5, g0, g1);
        const float pa = half_sum(sa_.x + sa_.y), pc2 = half_sum(sc_.x + sc_.y);
        if (hl == 0) *(f32x2*)(lds + L_MOD + 12288 + ((a & 1) * 2 + (lane >> 5)) * 8) = (f32x2){pa, pc2};
      }
      STAMP(9);
      if (arrive(cnt, 13) != 3) await<1>(cnt, 13, 4);
    }
    f32x2 ra[8], cb1[8], sb[8], gb[8];
    float suma, sumc;
    {
      const char* mp = lds + L_MOD + hl * 16;
      auto vec16 = [&](int v, f32x2* o) __attribute__((always_inline)) {
#pragma unroll
        for (int i = 0; i < 4; ++i) {
          const f32x4 t = *(const f32x4*)(mp + v * 2048 + (i >> 1) * 512 + (i & 1) * 1024);
          o[2 * i] = (f32x2){t[0], t[1]}; o[2 * i + 1] = (f32x2){t[2], t[3]};
        }
      };
      f32x2 a1[8], a0[8];
      vec16(0, a1); vec16(1, a0); vec16(2, ra); vec16(3, cb1); vec16(4, sb); vec16(5, gb);
      const f32x4 pa = *(const f32x4*)(lds + L_MOD + 12288), pc2 = *(const f32x4*)(lds + L_MOD + 12288 + 16);
#pragma unroll
      for (int e = 0; e < 8; ++e) gah[e] = gah[e] * a1[e] + a0[e];
      suma = (pa[0] + pa[2]) + (pc2[0] + pc2[2]);
      sumc = (pa[1] + pa[3]) + (pc2[1] + pc2[3]);
      PIN8(gah); PIN8(ra); PIN8(cb1); PIN8(sb); PIN8(gb); PIN(suma); PIN(sumc);
    }
    int o_key = hl < 8 ? OFF_RK + hl * 256 * RKS + j * RKS + (row & 7) * 4 : OOB;
    PIN(o_key);
    STAMP(10);
    bar();
    STAMP(11);
    {
      f32x2 z[8];
      const char* zr = lds + L_Z + (2 * a + (lane >> 5)) * 1040 + hl * 16;
      unpack16(*(const u32x4*)(zr), *(const u32x4*)(zr + 512), z);
      f32x2 s1 = {0, 0}, s2 = {0, 0}, s3 = {0, 0};
#pragma unroll
      for (int e = 0; e < 8; ++e) { x[e] += gah[e] * z[e]; s1 += x[e]; s2 += x[e] * x[e]; s3 += x[e] * ra[e]; }
      const float t1 = half_sum(s1.x + s1.y), t2 = half_sum(s2.x + s2.y), t3 = half_sum(s3.x + s3.y);
      const float mean = t1 * (1.f / 512.f);
      const f32x2 mvb = rb2((f32x2){mean, fmaxf(t2 * (1.f / 512.f) - mean * mean, 0.f)});
      const float mb = mvb.x, isd = __builtin_amdgcn_rcpf(rb(__builtin_amdgcn_sqrtf(rb(mvb.y + 1e-5f))));
      const float rr = (t3 - mb * suma) * isd + sumc;
      u32 kb = pk(0.f, rr);
      kb ^= ((int)kb < 0) ? 0xffff0000u : 0x80000000u;
      __builtin_amdgcn_raw_buffer_store_b32(kb | 0x8000u | (u32)(2047 - row), rW, o_key, 0, SC1);
      signal(cnt, 10);
      STAMP(12);
      u32 hw[8], ow[8];
#pragma unroll
      for (int e = 0; e < 8; ++e) {
        const f32x2 h2 = (x[e] - mb) * isd * cb1[e] + sb[e];
        hw[e] = nos(pk2(h2));
        ow[e] = pk2(x[e] + gb[e] * h2);
      }
      st128((u32x4){hw[0], hw[1], hw[2], hw[3]}, rW, OFF_H2 + row * 1024 + hl * 16);
      st128((u32x4){hw[4], hw[5], hw[6], hw[7]}, rW, OFF_H2 + row * 1024 + 512 + hl * 16);
      PIN8(ow);
      STAMP(13);
      if (jobF || jobG) bar(); else __builtin_amdgcn_s_sleep(30);
      st128((u32x4){ow[0], ow[1], ow[2], ow[3]}, rO, row * 1024 + hl * 16);
      st128((u32x4){ow[4], ow[5], ow[6], ow[7]}, rO, row * 1024 + 512 + hl * 16);
    }
    if (b == 0 && a == 0) __builtin_amdgcn_raw_buffer_store_b64(tagv, rW, lane == 0 ? OFF_EP : OOB, 0, SC1);
    if (jobF || jobG) bar();
    drain();
    STAMP(14);
    if (arrive(cnt, 5) == 3) __builtin_amdgcn_raw_buffer_store_b64(tagv, rW, lane == 0 ? OFF_FL3 + j * 128 : OOB, 0, SC1);
    if (jobG && a < 2) {
      __builtin_amdgcn_s_setprio(0);
      bar();
      const int r = lane & 15, tok = at >> 4;
      const u32 key = *(const u32*)(lds + L_SEL + tok * 4);
      const int orow = 2047 - (int)(key & 0x7ffu);
#pragma unroll 1
      for (int spins = 0; spins < SPIN_CAP; ++spins) {
        FENCE();
        const u32x2 f = __builtin_amdgcn_raw_buffer_load_b64(rW, OFF_FL3 + (orow >> 3) * 128, 0, SC1);
        if (__all(f.x == tagv.x && f.y == tagv.y)) break;
        __builtin_amdgcn_s_sleep(4);
      }
      FENCE();
      float oold = lo2f(__builtin_amdgcn_raw_buffer_load_b16(rO, orow * 1024 + (gd0 + r) * 2, 0, SC1));
      u32 rbits = key >> 16;
      rbits = (rbits & 0x8000u) ? (rbits & 0x7fffu) : (rbits ^ 0xffffu);
      float gy = rb(__builtin_amdgcn_rcpf(1.f + __expf(-lo2f(rbits)))) * *(const float*)(lds + L_MOD + 10240 + ((r >> 2) & 1) * 1024 + ((gd0 + r) >> 3) * 16 + (r & 3) * 4);
      int oo = (r & 1) ? OOB : orow * 1024 + (gd0 + r) * 2, po = L_KC + ((tok >> 2) * 16 + r) * 16 + (tok & 3) * 4;
      PIN(gy); PIN(oold); PIN(oo); PIN(po);
      STAMP(15);
      bar();
      float y = 0;
#pragma unroll
      for (int i = 0; i < 8; ++i) y += *(const float*)(lds + po + i * 512);
      const float o = oold + gy * rb(y);
      const float on = dppf<DPP_X1>(o);
      __builtin_amdgcn_raw_buffer_store_b32(pk(o, on), rO, oo, 0, SC1);
      STAMP(16);
    }
    FLUSH();
  }
}

extern "C" void kernel_launch(void* const* d_in, const int* in_sizes, int n_in, void* d_out, int out_size, void* d_ws,
                              size_t ws_size, hipStream_t stream) {
  fused_kernel<<<256, 512, 0, stream>>>((char*)d_ws + (WSEND ? ((ws_size - WS_BYTES) & ~(size_t)4095) : 0), desc_hi(WS_BYTES), (const u16*)d_in[1], desc_hi(S * D * 2), (const u16*)d_in[2],
                                        desc_hi(S * D * 2), (const u16*)d_in[3], desc_hi(D * 8), (const u16*)d_in[4], (const u16*)d_in[11],
                                        (const u16*)d_in[12], (const u16*)d_in[0], (const u16*)d_in[5], (const u16*)d_in[7],
                                        (const u16*)d_in[8], (const u16*)d_in[9], (const u16*)d_in[10], (u16*)d_out);
}
```
